```python
import torch

def _softmax(x, axis=-1):
    return torch.softmax(x, dim=axis)
def _gelu(x):
    return torch.nn.functional.gelu(x, approximate="tanh")
def _silu(x):
    return torch.nn.functional.silu(x)
def _sigmoid(x):
    return torch.sigmoid(x)
def _layernorm(x, axis=-1, eps=1e-5):
    m = x.mean(dim=axis, keepdim=True)
    v = x.var(dim=axis, keepdim=True, unbiased=False)
    return (x - m) / torch.sqrt(v + eps)
def _scatter_add(tgt, idx, src, axis=0):
    return tgt.clone().index_add_(axis, idx, src)
def _segment_sum(src, seg, n):
    out = torch.zeros((n,) + src.shape[1:], dtype=src.dtype)
    return out.index_add_(0, seg, src)
def _pad(x, pw):
    flat = tuple(v for pair in reversed(pw) for v in pair)
    return torch.nn.functional.pad(x, flat)

kernel_name = 'gather_scatter__cheap_equivariant_egnn__graph'

def setup_inputs():

    _w = (lambda a: (lambda t: t.to(torch.bfloat16) if t.is_floating_point() else t)(torch.as_tensor(a)))
    N, deg, D, De = 10000, 16, 128, max(4, 1)
    E = N * deg
    return {
        "h": _w((lambda *s: torch.randn(*s, dtype=torch.bfloat16) * 0.2)(N, D)),
        "pos": _w((lambda *s: torch.randn(*s, dtype=torch.bfloat16) * 0.8)(N, 3)),
        "e_attr": _w((lambda *s: torch.randn(*s, dtype=torch.bfloat16) * 0.8)(E, De)),
        "src": _w((lambda lo, hi, *s: torch.randint(lo, hi, s))(0, N, E).to(torch.int64)),
        "dst": _w((lambda lo, hi, *s: torch.randint(lo, hi, s))(0, N, E).to(torch.int64)),
        "w_e": _w(0.1 * (lambda *s: torch.randn(*s, dtype=torch.bfloat16) * 0.8)(2 * D + 1 + De, D)),
        "w_x": _w(0.1 * (lambda *s: torch.randn(*s, dtype=torch.bfloat16) * 0.2)(D, 1)),
        "w_h": _w(0.1 * (lambda *s: torch.randn(*s, dtype=torch.bfloat16) * 0.2)(2 * D, D)),
    }

def reference(h, pos, e_attr, src, dst, w_e, w_x, w_h):
    N, D = h.shape[0], h.shape[1]
    diff = pos[dst] - pos[src]
    dist2 = torch.sum(diff * diff, axis=-1, keepdims=True)
    e_in = torch.concatenate([h[dst], h[src], dist2, e_attr], axis=-1)
    m_ij = _silu(e_in @ w_e)
    coord_scale = m_ij @ w_x
    pos_new = pos + _segment_sum(diff * coord_scale, dst, N)
    m_i = _segment_sum(m_ij, dst, N)
    h_new = h + _silu(torch.concatenate([h, m_i], axis=-1) @ w_h)
    return h_new, pos_new
```

```cpp
#include <hip/hip_runtime.h>
#include <hip/hip_bf16.h>
#include <stdint.h>
#include <stdio.h>

typedef unsigned short u16;
__device__ __forceinline__ float bf2f(u16 v){ return __uint_as_float(((unsigned)v)<<16); }
__device__ __forceinline__ u16 f2bf(float f){ __hip_bfloat16 b = __float2bfloat16(f); return *reinterpret_cast<u16*>(&b); }

__global__ void pq_kernel(const u16* h, const u16* we, float* P, float* Q, int N){
  int n = blockIdx.x; int c = threadIdx.x;
  if(n>=N) return;
  float p=0,q=0;
  for(int k=0;k<128;k++){ float hv=bf2f(h[n*128+k]); p+=hv*bf2f(we[k*128+c]); q+=hv*bf2f(we[(128+k)*128+c]); }
  P[n*128+c]=p; Q[n*128+c]=q;
}
__global__ void zero_kernel(float* a, int n){ int i=blockIdx.x*blockDim.x+threadIdx.x; if(i<n) a[i]=0; }
__global__ void edge_kernel(const u16* pos, const u16* ea, const int* src, const int* dst, const u16* we, const u16* wx,
   const float* P, const float* Q, float* macc, float* pacc, int E){
  int e = blockIdx.x; int c = threadIdx.x;
  if(e>=E) return;
  int s=src[e], d=dst[e];
  float df[3]; float d2=0;
  for(int k=0;k<3;k++){ float x = bf2f(f2bf(bf2f(pos[d*3+k])-bf2f(pos[s*3+k]))); df[k]=x; d2+=bf2f(f2bf(x*x)); }
  d2=bf2f(f2bf(d2));
  float z = P[d*128+c]+Q[s*128+c]+d2*bf2f(we[256*128+c]);
  for(int k=0;k<4;k++) z+=bf2f(ea[e*4+k])*bf2f(we[(257+k)*128+c]);
  z=bf2f(f2bf(z));
  float m = z/(1.f+__expf(-z));
  m=bf2f(f2bf(m));
  atomicAdd(&macc[d*128+c], m);
  __shared__ float red[128];
  red[c]=m*bf2f(wx[c]);
  __syncthreads();
  if(c==0){ float cs=0; for(int i=0;i<128;i++) cs+=red[i]; cs=bf2f(f2bf(cs)); for(int k=0;k<3;k++) atomicAdd(&pacc[d*4+k], bf2f(f2bf(df[k]*cs))); }
}
__global__ void node_kernel(const u16* h, const u16* pos, const u16* wh, const float* macc, const float* pacc, u16* out, int N){
  int n = blockIdx.x; int c = threadIdx.x;
  float z=0;
  for(int k=0;k<128;k++) z+=bf2f(h[n*128+k])*bf2f(wh[k*128+c]);
  for(int k=0;k<128;k++) z+=bf2f(f2bf(macc[n*128+k]))*bf2f(wh[(128+k)*128+c]);
  z=bf2f(f2bf(z));
  float s=z/(1.f+__expf(-z));
  out[n*128+c]=f2bf(bf2f(h[n*128+c])+bf2f(f2bf(s)));
  if(c<3) out[N*128+n*3+c]=f2bf(bf2f(pos[n*3+c])+bf2f(f2bf(pacc[n*4+c])));
}

extern "C" void kernel_launch(void* const* d_in, const int* in_sizes, int n_in,
                              void* d_out, int out_size, void* d_ws, size_t ws_size,
                              hipStream_t stream) {
  const u16* h=(const u16*)d_in[0]; const u16* pos=(const u16*)d_in[1]; const u16* ea=(const u16*)d_in[2];
  const int* src=(const int*)d_in[3]; const int* dst=(const int*)d_in[4];
  const u16* we=(const u16*)d_in[5]; const u16* wx=(const u16*)d_in[6]; const u16* wh=(const u16*)d_in[7];
  int N=in_sizes[0]/128; int E=in_sizes[3];
  float* P=(float*)d_ws; float* Q=P+(size_t)N*128; float* macc=Q+(size_t)N*128; float* pacc=macc+(size_t)N*128;
  zero_kernel<<<(N*132+255)/256,256,0,stream>>>(macc,N*132);
  pq_kernel<<<N,128,0,stream>>>(h,we,P,Q,N);
  edge_kernel<<<E,128,0,stream>>>(pos,ea,src,dst,we,wx,P,Q,macc,pacc,E);
  node_kernel<<<N,128,0,stream>>>(h,pos,wh,macc,pacc,(u16*)d_out,N);
}
```

```cpp
#include <hip/hip_runtime.h>
#include <stdint.h>

typedef unsigned short u16;
typedef unsigned int u32;
typedef __bf16 bf16x8 __attribute__((ext_vector_type(8)));
typedef short s16x4 __attribute__((ext_vector_type(4)));
typedef float f32x4 __attribute__((ext_vector_type(4)));

#define D_ 128
constexpr int NB   = 40;
constexpr int CH   = 2048;
constexpr int K1T  = 512;
constexpr int K2T  = 1024;
constexpr int CAP  = 1024;
constexpr int MAXNP = 4096;

__device__ __forceinline__ float bf2f(u16 v){ return __uint_as_float(((u32)v)<<16); }
__device__ __forceinline__ u16 f2bf(float f){
  u32 u = __float_as_uint(f);
  u32 r = (u + 0x7FFFu + ((u>>16)&1u)) >> 16;
  if ((u & 0x7FFFFFFFu) > 0x7F800000u) r = (u>>16) | 0x40u;
  return (u16)r;
}
__device__ __forceinline__ float rbf(float f){ return bf2f(f2bf(f)); }
__device__ __forceinline__ u32 pk2(float a, float b){ return (u32)f2bf(a) | ((u32)f2bf(b)<<16); }
__device__ __forceinline__ float silu_f(float z){ return z * __builtin_amdgcn_rcpf(1.0f + __builtin_amdgcn_exp2f(-1.4426950408889634f * z)); }

union U4 { uint4 u; bf16x8 b; u16 h[8]; };

__global__ __launch_bounds__(K1T) void k1_kernel(const u16* __restrict__ h, const u16* __restrict__ pos,
    const u16* __restrict__ ea, const int* __restrict__ src, const int* __restrict__ dst,
    const u16* __restrict__ we, const u16* __restrict__ wh,
    float* __restrict__ P, u16* __restrict__ Qb, uint4* __restrict__ entF, uint2* __restrict__ entI,
    u32* __restrict__ offT, uint4* __restrict__ whF, int N, int E, int nchunk, int ngemm, int np)
{
  __shared__ __attribute__((aligned(16))) char smem[256*68*4];
  const int tid = threadIdx.x;
  const int b = blockIdx.x;
  if (b < nchunk) {
    u32* hist = (u32*)smem;
    for (int i = tid; i <= np; i += K1T) hist[i] = 0;
    __syncthreads();
    const int base = b * CH;
    int bin[4], slot[4]; uint4 F[4]; uint2 I[4];
#pragma unroll
    for (int k = 0; k < 4; k++) {
      int e = base + k*K1T + tid;
      bin[k] = -1;
      if (e < E) {
        int d = dst[e], s = src[e];
        uint2 a = *(const uint2*)(ea + (size_t)e*4);
        float dx = rbf(bf2f(pos[d*3+0]) - bf2f(pos[s*3+0]));
        float dy = rbf(bf2f(pos[d*3+1]) - bf2f(pos[s*3+1]));
        float dz = rbf(bf2f(pos[d*3+2]) - bf2f(pos[s*3+2]));
        float d2 = rbf(dx*dx) + rbf(dy*dy) + rbf(dz*dz);
        u32 d2b = f2bf(d2), dxb = f2bf(dx), dyb = f2bf(dy), dzb = f2bf(dz);
        F[k].x = d2b | (a.x << 16);
        F[k].y = (a.x >> 16) | (a.y << 16);
        F[k].z = (a.y >> 16) | (dxb << 16);
        F[k].w = dyb | (dzb << 16);
        int bn = d / NB;
        bin[k] = bn;
        I[k].x = (u32)s | ((u32)(d - bn*NB) << 26);
        I[k].y = (u32)e;
        slot[k] = (int)atomicAdd(&hist[bn], 1u);
      }
    }
    __syncthreads();
    if (tid < 64) {
      int nper = (np + 63) >> 6;
      int lo = tid * nper, hi = min(np, lo + nper);
      u32 s = 0;
      for (int i = lo; i < hi; i++) s += hist[i];
      u32 incl = s;
      for (int o = 1; o < 64; o <<= 1) { u32 v = __shfl_up(incl, o); if (tid >= o) incl += v; }
      u32 run = incl - s;
      for (int i = lo; i < hi; i++) { u32 c = hist[i]; hist[i] = run; run += c; }
      if (tid == 63) hist[np] = incl;
    }
    __syncthreads();
    for (int i = tid; i <= np; i += K1T) offT[(size_t)i * nchunk + b] = hist[i];
#pragma unroll
    for (int k = 0; k < 4; k++) {
      if (bin[k] >= 0) {
        size_t idx = (size_t)base + hist[bin[k]] + slot[k];
        entF[idx] = F[k];
        entI[idx] = I[k];
      }
    }
  } else if (b < nchunk + ngemm) {
    u32* Wt32 = (u32*)smem;
    for (int it = tid; it < 2048; it += K1T) {
      int kp = it & 63, oct = (it >> 6) & 15, half = it >> 10;
      int k = half*128 + 2*kp;
      U4 r0, r1;
      r0.u = *(const uint4*)(we + k*128 + oct*8);
      r1.u = *(const uint4*)(we + (k+1)*128 + oct*8);
#pragma unroll
      for (int i = 0; i < 8; i++)
        Wt32[(half*128 + i*16 + oct)*68 + kp] = (u32)r0.h[i] | ((u32)r1.h[i] << 16);
    }
    __syncthreads();
    const int w = tid >> 6, l = tid & 63, g = l >> 4, j = l & 15;
    const int half = w & 1, rg = w >> 1;
    const int rowbase = (b - nchunk) * 128 + rg * 32;
    f32x4 acc[2][8];
#pragma unroll
    for (int rt = 0; rt < 2; rt++)
#pragma unroll
      for (int t = 0; t < 8; t++) acc[rt][t] = (f32x4){0.f,0.f,0.f,0.f};
    U4 A[2][4];
#pragma unroll
    for (int rt = 0; rt < 2; rt++) {
      int row = min(rowbase + rt*16 + j, N-1);
#pragma unroll
      for (int s = 0; s < 4; s++) A[rt][s].u = *(const uint4*)(h + (size_t)row*128 + 32*s + 8*g);
    }
#pragma unroll
    for (int s = 0; s < 4; s++) {
#pragma unroll
      for (int t = 0; t < 8; t++) {
        U4 B; B.u = *(const uint4*)(Wt32 + (half*128 + t*16 + j)*68 + 16*s + 4*g);
#pragma unroll
        for (int rt = 0; rt < 2; rt++)
          acc[rt][t] = __builtin_amdgcn_mfma_f32_16x16x32_bf16(A[rt][s].b, B.b, acc[rt][t], 0, 0, 0);
      }
    }
#pragma unroll
    for (int rt = 0; rt < 2; rt++) {
#pragma unroll
      for (int r = 0; r < 4; r++) {
        int row = rowbase + rt*16 + 4*g + r;
        if (row < N) {
          if (half == 0) {
            u32 hl[8];
#pragma unroll
            for (int t = 0; t < 8; t++) { float v = acc[rt][t][r]; u16 hi = f2bf(v); u16 lo = f2bf(v - bf2f(hi)); hl[t] = (u32)hi | ((u32)lo << 16); }
            *(uint4*)(P + (size_t)row*128 + 8*j) = make_uint4(hl[0], hl[1], hl[2], hl[3]);
            *(uint4*)(P + (size_t)row*128 + 8*j + 4) = make_uint4(hl[4], hl[5], hl[6], hl[7]);
          } else {
            uint4 v;
            v.x = pk2(acc[rt][0][r], acc[rt][1][r]); v.y = pk2(acc[rt][2][r], acc[rt][3][r]);
            v.z = pk2(acc[rt][4][r], acc[rt][5][r]); v.w = pk2(acc[rt][6][r], acc[rt][7][r]);
            *(uint4*)(Qb + (size_t)row*128 + 8*j) = v;
          }
        }
      }
    }
  } else {
    for (int it = tid; it < 4096; it += K1T) {
      int l = it & 63, f = it >> 6, t = f & 7, s = f >> 3, g = l >> 4, j = l & 15;
      U4 v;
#pragma unroll
      for (int jj = 0; jj < 8; jj++) v.h[jj] = wh[(32*s + 8*g + jj)*128 + 8*j + t];
      whF[it] = v.u;
    }
  }
}

struct K2Smem {
  float sP[NB*128];
  uint4 sF[CAP];
  float part[(NB+16)*132];
  u32   sSrc[CAP];
  u32   tmpE[CAP];
  u16   sM[48*136];
  u32   cpre[264];
  u32   coff[264];
  int   ncount[64];
  int   nstart[72];
  unsigned char sNode[CAP];
};

__global__ __launch_bounds__(K2T) void k2_kernel(const u16* __restrict__ h, const u16* __restrict__ pos,
    const u16* __restrict__ ea, const int* __restrict__ src, const int* __restrict__ dst,
    const u16* __restrict__ we, const u16* __restrict__ wx,
    const float* __restrict__ P, const u16* __restrict__ Qb, const uint4* __restrict__ entF,
    const uint2* __restrict__ entI, const u32* __restrict__ offT, const uint4* __restrict__ whF,
    u16* __restrict__ out, int N, int E, int nchunk)
{
  __shared__ __attribute__((aligned(16))) K2Smem sm;
  const int tid = threadIdx.x, w = tid >> 6, l = tid & 63, g = l >> 4, j = l & 15;
  const int blk = blockIdx.x;
  const int n0 = blk * NB;
  const int nn = min(NB, N - n0);

  for (int i = tid; i < NB*32; i += K2T) {
    int n = i >> 5;
    float4 v = make_float4(0.f,0.f,0.f,0.f);
    if (n < nn) v = *(const float4*)(P + (size_t)(n0)*128 + (size_t)i*4);
    *(float4*)(sm.sP + i*4) = v;
  }
  if (tid < 64) sm.ncount[tid] = 0;
  if (w == 0) {
    u32 run = 0;
    for (int c0 = 0; c0 < nchunk; c0 += 64) {
      int c = c0 + l;
      u32 o0 = 0, o1 = 0;
      if (c < nchunk) { o0 = offT[(size_t)blk*nchunk + c]; o1 = offT[(size_t)(blk+1)*nchunk + c]; }
      u32 len = o1 - o0, incl = len;
      for (int o = 1; o < 64; o <<= 1) { u32 v = __shfl_up(incl, o); if (l >= o) incl += v; }
      if (c < nchunk) { sm.cpre[c] = run + incl - len; sm.coff[c] = o0; }
      run += __shfl(incl, 63);
    }
    if (l == 0) sm.cpre[nchunk] = run;
  }
  __syncthreads();
  const int T = (int)sm.cpre[nchunk];
  const bool fast = (T <= CAP);

  union S4 { u32 u[2]; s16x4 v; u16 h[4]; };
  S4 W5[8];
#pragma unroll
  for (int t = 0; t < 8; t++) {
    int c = 8*j + t;
    W5[t].u[0] = 0; W5[t].u[1] = 0;
    if (g == 0) {
#pragma unroll
      for (int k = 0; k < 4; k++) W5[t].h[k] = we[(256+k)*128 + c];
    } else if (g == 1) W5[t].h[0] = we[260*128 + c];
  }

  if (fast) {
    uint4 F; u32 sv = 0, ev = 0; int dl = -1, slot = 0;
    if (tid < T) {
      int lo = 0, hi = nchunk - 1;
      while (lo < hi) { int mid = (lo + hi + 1) >> 1; if ((int)sm.cpre[mid] <= tid) lo = mid; else hi = mid - 1; }
      size_t idx = (size_t)lo * CH + sm.coff[lo] + (tid - sm.cpre[lo]);
      F = entF[idx]; uint2 I = entI[idx];
      sv = I.x & 0x3FFFFFFu; dl = I.x >> 26; ev = I.y;
      slot = atomicAdd(&sm.ncount[dl], 1);
    }
    __syncthreads();
    if (w == 0) {
      int c = (l < NB) ? sm.ncount[l] : 0, incl = c;
      for (int o = 1; o < 64; o <<= 1) { int v = __shfl_up(incl, o); if (l >= o) incl += v; }
      sm.nstart[l] = incl - c;
      if (l == 63) sm.nstart[64] = incl;
    }
    __syncthreads();
    if (tid < T) sm.tmpE[sm.nstart[dl] + slot] = ev;
    __syncthreads();
    if (tid < T) {
      int a = sm.nstart[dl], bnd = sm.nstart[dl+1], rank = 0;
      for (int i = a; i < bnd; i++) rank += (sm.tmpE[i] < ev) ? 1 : 0;
      int p = a + rank;
      sm.sF[p] = F; sm.sSrc[p] = sv; sm.sNode[p] = (unsigned char)dl;
    }
    __syncthreads();

    const int per = ((T + 15) / 16 + 3) & ~3;
    int p0 = min(T, w * per), pend = min(T, (w+1) * per);
    p0 = __builtin_amdgcn_readfirstlane(p0); pend = __builtin_amdgcn_readfirstlane(pend);
    while (p0 < pend) {
      int n = __builtin_amdgcn_readfirstlane((int)sm.sNode[p0]);
      int s1 = min(pend, __builtin_amdgcn_readfirstlane(sm.nstart[n+1]));
      f32x4 acc2[8];
#pragma unroll
      for (int t = 0; t < 8; t++) acc2[t] = (f32x4){0.f,0.f,0.f,0.f};
      if (g == 2) {
        uint4 pa = *(const uint4*)((const u32*)sm.sP + n*128 + 8*j);
        uint4 pb = *(const uint4*)((const u32*)sm.sP + n*128 + 8*j + 4);
        W5[0].u[0] = pa.x; W5[1].u[0] = pa.y; W5[2].u[0] = pa.z; W5[3].u[0] = pa.w;
        W5[4].u[0] = pb.x; W5[5].u[0] = pb.y; W5[6].u[0] = pb.z; W5[7].u[0] = pb.w;
      }
      for (int tb = p0; tb < s1; tb += 16) {
        const int len = min(16, s1 - tb);
        S4 A; A.u[0] = 0; A.u[1] = 0;
        if (g < 2) { int p = min(4*(j&3) + (j>>2), len-1); uint2 v = *(const uint2*)((const u32*)&sm.sF[tb + p] + 2*g); A.u[0] = v.x; A.u[1] = v.y; }
        else if (g == 2) A.u[0] = 0x3F803F80u;
        U4 q[4];
#pragma unroll
        for (int r = 0; r < 4; r++) {
          int p = min(4*r + g, len-1);
          u32 s = sm.sSrc[tb + p];
          q[r].u = *(const uint4*)(Qb + (size_t)s*128 + 8*j);
        }
        s16x4 A2;
        {
          int qs = j >> 2;
#pragma unroll
          for (int jj = 0; jj < 4; jj++) {
            int p = 4*jj + g;
            u16 v = 0;
            if ((j & 3) == 0 && p < len) v = (qs == 0) ? (u16)0x3F80 : ((const u16*)&sm.sF[tb + p])[4 + qs];
            A2[jj] = (short)v;
          }
        }
        const int nst = (len + 3) >> 2;
#pragma unroll
        for (int th = 0; th < 2; th++) {
          f32x4 z[4];
#pragma unroll
          for (int t = 0; t < 4; t++) z[t] = __builtin_amdgcn_mfma_f32_16x16x16bf16_1k(A.v, W5[th*4+t].v, (f32x4){0.f,0.f,0.f,0.f}, 0, 0, 0);
#pragma unroll
          for (int r = 0; r < 4; r++) {
            if (r < nst) {
#pragma unroll
              for (int t = 0; t < 4; t++) z[t][r] = silu_f(z[t][r] + bf2f(q[r].h[th*4+t]));
            } else {
#pragma unroll
              for (int t = 0; t < 4; t++) z[t][r] = 0.f;
            }
          }
#pragma unroll
          for (int t = 0; t < 4; t++) {
            S4 B2;
            B2.u[0] = pk2(z[t][0], z[t][1]); B2.u[1] = pk2(z[t][2], z[t][3]);
            acc2[th*4+t] = __builtin_amdgcn_mfma_f32_16x16x16bf16_1k(A2, B2.v, acc2[th*4+t], 0, 0, 0);
          }
        }
      }
      float* pt = sm.part + (n + w) * 132;
      if (g == 0) {
        *(float4*)(pt + 8*j)     = make_float4(acc2[0][0], acc2[1][0], acc2[2][0], acc2[3][0]);
        *(float4*)(pt + 8*j + 4) = make_float4(acc2[4][0], acc2[5][0], acc2[6][0], acc2[7][0]);
      } else {
        float s = 0.f;
#pragma unroll
        for (int t = 0; t < 8; t++) s += acc2[t][0] * bf2f(wx[8*j + t]);
        s += __shfl_xor(s, 1); s += __shfl_xor(s, 2); s += __shfl_xor(s, 4); s += __shfl_xor(s, 8);
        if (j == 0) pt[128 + g - 1] = s;
      }
      p0 = s1;
    }
    __syncthreads();
    for (int i = tid; i < 48*32; i += K2T) {
      int n = i >> 5, c4 = (i & 31) * 4;
      float4 s = make_float4(0.f,0.f,0.f,0.f);
      if (n < nn) {
        int a = sm.nstart[n], bnd = sm.nstart[n+1];
        if (bnd > a) {
          int wl = a / per, wh_ = (bnd - 1) / per;
          for (int ww = wl; ww <= wh_; ww++) {
            float4 v = *(const float4*)(sm.part + (n + ww)*132 + c4);
            s.x += v.x; s.y += v.y; s.z += v.z; s.w += v.w;
          }
        }
      }
      uint2 o; o.x = pk2(s.x, s.y); o.y = pk2(s.z, s.w);
      *(uint2*)(sm.sM + n*136 + c4) = o;
    }
    if (tid < nn*3) {
      int n = tid / 3, k = tid - n*3;
      float s = 0.f;
      int a = sm.nstart[n], bnd = sm.nstart[n+1];
      if (bnd > a) {
        int wl = a / per, wh_ = (bnd - 1) / per;
        for (int ww = wl; ww <= wh_; ww++) s += sm.part[(n + ww)*132 + 128 + k];
      }
      out[(size_t)N*128 + (size_t)(n0+n)*3 + k] = f2bf(bf2f(pos[(n0+n)*3 + k]) + s);
    }
  } else {
    for (int n = w; n < 48; n += 16) {
      float a0 = 0.f, a1 = 0.f, px = 0.f, py = 0.f, pz = 0.f;
      if (n < nn) {
        const int node = n0 + n;
        const u32 pu0 = ((const u32*)sm.sP)[n*128 + 2*l], pu1 = ((const u32*)sm.sP)[n*128 + 2*l + 1];
        const float p0v = bf2f((u16)(pu0 & 0xFFFF)) + bf2f((u16)(pu0 >> 16)), p1v = bf2f((u16)(pu1 & 0xFFFF)) + bf2f((u16)(pu1 >> 16));
        const float wx0 = bf2f(wx[2*l]), wx1 = bf2f(wx[2*l+1]);
        float w50[5], w51[5];
        for (int k = 0; k < 5; k++) { w50[k] = bf2f(we[(256+k)*128 + 2*l]); w51[k] = bf2f(we[(256+k)*128 + 2*l+1]); }
        for (int e0 = 0; e0 < E; e0 += 64) {
          int e = e0 + l;
          int d = (e < E) ? dst[e] : -1;
          unsigned long long m = __ballot(d == node);
          while (m) {
            int bit = __ffsll((long long)m) - 1; m &= m - 1;
            int ee = e0 + bit; int s = src[ee];
            float dx = rbf(bf2f(pos[node*3+0]) - bf2f(pos[s*3+0]));
            float dy = rbf(bf2f(pos[node*3+1]) - bf2f(pos[s*3+1]));
            float dz = rbf(bf2f(pos[node*3+2]) - bf2f(pos[s*3+2]));
            float d2 = rbf(rbf(dx*dx) + rbf(dy*dy) + rbf(dz*dz));
            u32 qq = *(const u32*)(Qb + (size_t)s*128 + 2*l);
            float z0 = p0v + d2*w50[0], z1 = p1v + d2*w51[0];
            for (int k = 0; k < 4; k++) { float f = bf2f(ea[(size_t)ee*4+k]); z0 += f*w50[1+k]; z1 += f*w51[1+k]; }
            z0 += bf2f((u16)(qq & 0xFFFF)); z1 += bf2f((u16)(qq >> 16));
            float m0 = rbf(silu_f(z0)), m1 = rbf(silu_f(z1));
            a0 += m0; a1 += m1;
            float cs = m0*wx0 + m1*wx1;
            px += dx*cs; py += dy*cs; pz += dz*cs;
          }
        }
        for (int o = 1; o < 64; o <<= 1) { px += __shfl_xor(px, o); py += __shfl_xor(py, o); pz += __shfl_xor(pz, o); }
        if (l == 0) {
          out[(size_t)N*128 + (size_t)node*3 + 0] = f2bf(bf2f(pos[node*3+0]) + px);
          out[(size_t)N*128 + (size_t)node*3 + 1] = f2bf(bf2f(pos[node*3+1]) + py);
          out[(size_t)N*128 + (size_t)node*3 + 2] = f2bf(bf2f(pos[node*3+2]) + pz);
        }
      }
      *(u32*)(sm.sM + n*136 + 2*l) = pk2(a0, a1);
    }
  }
  __syncthreads();
  float* sOut = sm.part;
  {
    const int t = w & 7;
    const int rt0 = (w >> 3) ? 2 : 0, nrt = (w >> 3) ? 1 : 2;
    f32x4 acc[2] = {(f32x4){0.f,0.f,0.f,0.f}, (f32x4){0.f,0.f,0.f,0.f}};
#pragma unroll
    for (int s = 0; s < 8; s++) {
      U4 B; B.u = whF[(s*8 + t)*64 + l];
#pragma unroll
      for (int i = 0; i < 2; i++) {
        if (i < nrt) {
          int rl = (rt0 + i)*16 + j;
          U4 A;
          if (s < 4) { int row = min(n0 + rl, N-1); A.u = *(const uint4*)(h + (size_t)row*128 + 32*s + 8*g); }
          else A.u = *(const uint4*)(sm.sM + rl*136 + 32*(s-4) + 8*g);
          acc[i] = __builtin_amdgcn_mfma_f32_16x16x32_bf16(A.b, B.b, acc[i], 0, 0, 0);
        }
      }
    }
    __syncthreads();
#pragma unroll
    for (int i = 0; i < 2; i++) if (i < nrt) {
#pragma unroll
      for (int r = 0; r < 4; r++) sOut[((rt0+i)*16 + 4*g + r)*132 + 8*j + t] = acc[i][r];
    }
  }
  __syncthreads();
  for (int i = tid; i < nn*16; i += K2T) {
    int n = i >> 4, c8 = (i & 15) * 8;
    U4 hv; hv.u = *(const uint4*)(h + (size_t)(n0+n)*128 + c8);
    U4 o;
#pragma unroll
    for (int k = 0; k < 8; k++) { float z = sOut[n*132 + c8 + k]; o.h[k] = f2bf(bf2f(hv.h[k]) + silu_f(z)); }
    *(uint4*)(out + (size_t)(n0+n)*128 + c8) = o.u;
  }
}

extern "C" void kernel_launch(void* const* d_in, const int* in_sizes, int n_in,
                              void* d_out, int out_size, void* d_ws, size_t ws_size,
                              hipStream_t stream) {
  const u16* h=(const u16*)d_in[0]; const u16* pos=(const u16*)d_in[1]; const u16* ea=(const u16*)d_in[2];
  const int* src=(const int*)d_in[3]; const int* dst=(const int*)d_in[4];
  const u16* we=(const u16*)d_in[5]; const u16* wx=(const u16*)d_in[6]; const u16* wh=(const u16*)d_in[7];
  int N = in_sizes[0]/128; int E = in_sizes[3];
  int nchunk = (E + CH - 1)/CH; int ngemm = (N + 127)/128; int np = (N + NB - 1)/NB;
  char* ws = (char*)d_ws; size_t o = 0;
  auto take = [&](size_t bytes){ void* p = ws + o; o += (bytes + 255) & ~(size_t)255; return p; };
  float* P = (float*)take((size_t)N*128*4);
  u16* Qb = (u16*)take((size_t)N*128*2);
  uint4* entF = (uint4*)take((size_t)nchunk*CH*16);
  uint2* entI = (uint2*)take((size_t)nchunk*CH*8);
  u32* offT = (u32*)take((size_t)(np+1)*nchunk*4);
  uint4* whF = (uint4*)take(65536);
  k1_kernel<<<nchunk + ngemm + 1, K1T, 0, stream>>>(h,pos,ea,src,dst,we,wh,P,Qb,entF,entI,offT,whF,N,E,nchunk,ngemm,np);
  k2_kernel<<<np, K2T, 0, stream>>>(h,pos,ea,src,dst,we,wx,P,Qb,entF,entI,offT,whF,(u16*)d_out,N,E,nchunk);
}
```

```cpp
#include <hip/hip_runtime.h>
#include <stdint.h>

typedef unsigned short u16;
typedef unsigned int u32;
typedef __bf16 bf16x8 __attribute__((ext_vector_type(8)));
typedef short s16x4 __attribute__((ext_vector_type(4)));
typedef float f32x4 __attribute__((ext_vector_type(4)));

constexpr int NB   = 40;
constexpr int CH   = 2048;
constexpr int CC   = 24;
constexpr int K1T  = 512;
constexpr int K2T  = 1024;
constexpr int CAP  = 1024;
constexpr int NS   = 12;

__device__ __forceinline__ float bf2f(u16 v){ return __uint_as_float(((u32)v)<<16); }
typedef __bf16 bf16x2 __attribute__((ext_vector_type(2)));
typedef float f32x2 __attribute__((ext_vector_type(2)));
__device__ __forceinline__ u32 pk2(float a, float b){
  f32x2 v = {a, b};
  bf16x2 r = __builtin_convertvector(v, bf16x2);
  return __builtin_bit_cast(u32, r);
}
__device__ __forceinline__ u16 f2bf(float f){ return (u16)(pk2(f, 0.f) & 0xFFFFu); }
__device__ __forceinline__ float rbf(float f){ return __uint_as_float(pk2(0.f, f) & 0xFFFF0000u); }
__device__ __forceinline__ float silu_f(float z){ return z * __builtin_amdgcn_rcpf(1.0f + __builtin_amdgcn_exp2f(-1.4426950408889634f * z)); }

union U4 { uint4 u; bf16x8 b; u16 h[8]; };
union S4 { u32 u[2]; s16x4 v; u16 h[4]; };

#define STAMP(k, idx)
__global__ __launch_bounds__(K1T) void k1_kernel(const u16* __restrict__ h, const u16* __restrict__ pos,
    const u16* __restrict__ ea, const int* __restrict__ src, const int* __restrict__ dst,
    const u16* __restrict__ we, const u16* __restrict__ wh,
    u32* __restrict__ P, u16* __restrict__ Qb, uint4* __restrict__ entF, uint2* __restrict__ entI,
    u32* __restrict__ cntT, uint4* __restrict__ whF, int N, int E, int nchunk, int ngemm, int np
)
{
  STAMP(0,0);
  __shared__ __attribute__((aligned(16))) char smem[256*68*4];
  const int tid = threadIdx.x;
  const int b = blockIdx.x;
  if (b < nchunk) {
    u32* hist = (u32*)smem;
    for (int i = tid; i < np; i += K1T) hist[i] = 0;
    const int base = b * CH;
    int dv[4], sv[4]; uint2 av[4];
#pragma unroll
    for (int k = 0; k < 4; k++) {
      int e = min(base + k*K1T + tid, E-1);
      dv[k] = dst[e]; sv[k] = src[e]; av[k] = *(const uint2*)(ea + (size_t)e*4);
    }
    u16 pd[4][3], ps[4][3];
#pragma unroll
    for (int k = 0; k < 4; k++) {
#pragma unroll
      for (int c = 0; c < 3; c++) { pd[k][c] = pos[dv[k]*3+c]; ps[k][c] = pos[sv[k]*3+c]; }
    }
    __syncthreads();
    STAMP(0,1);
#pragma unroll
    for (int k = 0; k < 4; k++) {
      int e = base + k*K1T + tid;
      int d = dv[k], s = sv[k]; uint2 a = av[k];
      float dx = rbf(bf2f(pd[k][0]) - bf2f(ps[k][0]));
      float dy = rbf(bf2f(pd[k][1]) - bf2f(ps[k][1]));
      float dz = rbf(bf2f(pd[k][2]) - bf2f(ps[k][2]));
      float d2 = rbf(dx*dx) + rbf(dy*dy) + rbf(dz*dz);
      u32 d2b = f2bf(d2), dxb = f2bf(dx), dyb = f2bf(dy), dzb = f2bf(dz);
      uint4 F; uint2 I;
      F.x = d2b | (a.x << 16);
      F.y = (a.x >> 16) | (a.y << 16);
      F.z = (a.y >> 16) | (dxb << 16);
      F.w = dyb | (dzb << 16);
      int bn = d / NB;
      I.x = (u32)s | ((u32)(d - bn*NB) << 26);
      I.y = (u32)e;
      if (e < E) {
        u32 slot = atomicAdd(&hist[bn], 1u);
        if (slot < CC) {
          size_t idx = ((size_t)bn * nchunk + b) * CC + slot;
          entF[idx] = F; entI[idx] = I;
        }
      }
    }
    __syncthreads();
    STAMP(0,2);
    for (int i = tid; i < np; i += K1T) cntT[(size_t)i * nchunk + b] = hist[i];
    STAMP(0,3);
  } else if (b < nchunk + ngemm) {
    u32* Wt32 = (u32*)smem;
    const int w = tid >> 6, l = tid & 63, g = l >> 4, j = l & 15;
    const int half = w & 1, rg = w >> 1;
    const int rowbase = (b - nchunk) * 128 + rg * 32;
    U4 A[2][4];
#pragma unroll
    for (int rt = 0; rt < 2; rt++) {
      int row = min(rowbase + rt*16 + j, N-1);
#pragma unroll
      for (int s = 0; s < 4; s++) A[rt][s].u = *(const uint4*)(h + (size_t)row*128 + 32*s + 8*g);
    }
    {
      U4 r0[4], r1[4];
#pragma unroll
      for (int i4 = 0; i4 < 4; i4++) {
        int it = tid + i4*K1T;
        int kp = it & 63, oct = (it >> 6) & 15, hf = it >> 10;
        int k = hf*128 + 2*kp;
        r0[i4].u = *(const uint4*)(we + k*128 + oct*8);
        r1[i4].u = *(const uint4*)(we + (k+1)*128 + oct*8);
      }
#pragma unroll
      for (int i4 = 0; i4 < 4; i4++) {
        int it = tid + i4*K1T;
        int kp = it & 63, oct = (it >> 6) & 15, hf = it >> 10;
#pragma unroll
        for (int i = 0; i < 8; i++)
          Wt32[(hf*128 + i*16 + oct)*68 + kp] = (u32)r0[i4].h[i] | ((u32)r1[i4].h[i] << 16);
      }
    }
    __syncthreads();
    STAMP(0,1);
    f32x4 acc[2][8];
#pragma unroll
    for (int rt = 0; rt < 2; rt++)
#pragma unroll
      for (int t = 0; t < 8; t++) acc[rt][t] = (f32x4){0.f,0.f,0.f,0.f};
#pragma unroll
    for (int s = 0; s < 4; s++) {
#pragma unroll
      for (int t = 0; t < 8; t++) {
        U4 B; B.u = *(const uint4*)(Wt32 + (half*128 + t*16 + j)*68 + 16*s + 4*g);
#pragma unroll
        for (int rt = 0; rt < 2; rt++)
          acc[rt][t] = __builtin_amdgcn_mfma_f32_16x16x32_bf16(A[rt][s].b, B.b, acc[rt][t], 0, 0, 0);
      }
    }
    STAMP(0,2);
#pragma unroll
    for (int rt = 0; rt < 2; rt++) {
#pragma unroll
      for (int r = 0; r < 4; r++) {
        int row = rowbase + rt*16 + 4*g + r;
        if (row < N) {
          if (half == 0) {
            u32 hl[8];
#pragma unroll
            for (int t = 0; t < 8; t++) { float v = acc[rt][t][r]; u16 hi = f2bf(v); u16 lo = f2bf(v - bf2f(hi)); hl[t] = (u32)hi | ((u32)lo << 16); }
            *(uint4*)(P + (size_t)row*128 + 8*j) = make_uint4(hl[0], hl[1], hl[2], hl[3]);
            *(uint4*)(P + (size_t)row*128 + 8*j + 4) = make_uint4(hl[4], hl[5], hl[6], hl[7]);
          } else {
            uint4 v;
            v.x = pk2(acc[rt][0][r], acc[rt][1][r]); v.y = pk2(acc[rt][2][r], acc[rt][3][r]);
            v.z = pk2(acc[rt][4][r], acc[rt][5][r]); v.w = pk2(acc[rt][6][r], acc[rt][7][r]);
            *(uint4*)(Qb + (size_t)row*128 + 8*j) = v;
          }
        }
      }
    }
    STAMP(0,3);
  } else {
    int it = (b - nchunk - ngemm) * K1T + tid;
    {
      int l = it & 63, f = it >> 6, t = f & 7, s = f >> 3, g = l >> 4, j = l & 15;
      U4 v;
#pragma unroll
      for (int jj = 0; jj < 8; jj++) v.h[jj] = wh[(32*s + 8*g + jj)*128 + 8*j + t];
      whF[it] = v.u;
    }
    STAMP(0,3);
  }
}

struct K2Smem {
  uint4 sW[4096];
  uint4 sA[CAP];
  u32   sP[NB*128];
  float part[(NB+16)*132];
  u16   sH[48*136];
  u16   sG[4][CAP];
  u32   sSrc[CAP];
  int   ncount[64];
  int   nstart[72];
  int   flag[4];
  unsigned char sNode[CAP/4];
};

__global__ __launch_bounds__(K2T) void k2_kernel(const u16* __restrict__ h, const u16* __restrict__ pos,
    const u16* __restrict__ ea, const int* __restrict__ src, const int* __restrict__ dst,
    const u16* __restrict__ we, const u16* __restrict__ wx,
    const u32* __restrict__ P, const u16* __restrict__ Qb, const uint4* __restrict__ entF,
    const uint2* __restrict__ entI, const u32* __restrict__ cntT, const uint4* __restrict__ whF,
    u16* __restrict__ out, int N, int E, int nchunk
)
{
  STAMP(1,0);
  __shared__ __attribute__((aligned(16))) K2Smem sm;
  const int tid = threadIdx.x, w = tid >> 6, l = tid & 63, g = l >> 4, j = l & 15;
  const int blk = blockIdx.x;
  const int n0 = blk * NB;
  const int nn = min(NB, N - n0);
  const int nslot = nchunk * CC;
  u16* const sM = (u16*)sm.sA;
  u32* const tmpE = (u32*)sm.part;

  uint4 F[2]; uint2 I[2]; u32 cn[2];
#pragma unroll
  for (int k = 0; k < 2; k++) {
    int i = min(tid + k*K2T, nslot - 1);
    int c = i / CC;
    F[k] = entF[(size_t)blk * nslot + i];
    I[k] = entI[(size_t)blk * nslot + i];
    cn[k] = cntT[(size_t)blk * nchunk + c];
  }
  uint4 pv[2];
#pragma unroll
  for (int k = 0; k < 2; k++) {
    int i = tid + k*K2T;
    pv[k] = make_uint4(0,0,0,0);
    if (i < nn*32) pv[k] = *(const uint4*)(P + (size_t)n0*128 + (size_t)i*4);
  }
  uint4 hv = make_uint4(0,0,0,0);
  if (tid < nn*16) hv = *(const uint4*)(h + (size_t)n0*128 + (size_t)tid*8);
  uint4 wv[4];
#pragma unroll
  for (int k = 0; k < 4; k++) wv[k] = whF[tid + k*K2T];
  S4 W0[2], W1[2]; float wxv[2];
#pragma unroll
  for (int hh = 0; hh < 2; hh++) {
    int c = 2*l + hh;
#pragma unroll
    for (int k = 0; k < 4; k++) W0[hh].h[k] = we[(256+k)*128 + c];
    W1[hh].u[0] = we[260*128 + c]; W1[hh].u[1] = 0;
    wxv[hh] = bf2f(wx[c]);
  }
  for (int i = tid; i < CAP; i += K2T) {
    sm.sA[i] = make_uint4(0,0,0,0); sm.sSrc[i] = 0;
    sm.sG[0][i] = 0; sm.sG[1][i] = 0; sm.sG[2][i] = 0; sm.sG[3][i] = 0;
  }
  if (tid < 64) sm.ncount[tid] = 0;
  if (tid < 4) sm.flag[tid] = 0;
#pragma unroll
  for (int k = 0; k < 4; k++) sm.sW[tid + k*K2T] = wv[k];
#pragma unroll
  for (int k = 0; k < 2; k++) { int i = tid + k*K2T; if (i < NB*32) *(uint4*)(sm.sP + i*4) = pv[k]; }
  if (tid < 48*16) *(uint4*)(sm.sH + (tid >> 4)*136 + (tid & 15)*8) = hv;
  __syncthreads();
  STAMP(1,1);
  int dl[2], slot[2]; bool val[2];
#pragma unroll
  for (int k = 0; k < 2; k++) {
    int i = tid + k*K2T;
    int kk = i % CC;
    val[k] = (i < nslot) && (kk < (int)min(cn[k], (u32)CC));
    if (i < nslot && cn[k] > (u32)CC) sm.flag[0] = 1;
    dl[k] = I[k].x >> 26; slot[k] = 0;
    if (val[k]) slot[k] = atomicAdd(&sm.ncount[dl[k]], 1);
  }
  __syncthreads();
  if (w == 0) {
    int c = (l < NB) ? sm.ncount[l] : 0;
    int cp = (c + 3) & ~3, incl = cp;
    for (int o = 1; o < 64; o <<= 1) { int v = __shfl_up(incl, o); if (l >= o) incl += v; }
    sm.nstart[l] = incl - cp;
    if (l == 63) { sm.nstart[64] = incl; if (incl > CAP) sm.flag[0] = 1; }
  }
  __syncthreads();
  const bool fast = (sm.flag[0] == 0);
  const int Tp = sm.nstart[64];
  int per = 0;
  if (fast) {
#pragma unroll
    for (int k = 0; k < 2; k++) if (val[k]) tmpE[sm.nstart[dl[k]] + slot[k]] = I[k].y;
    if (tid < NB) { int a = sm.nstart[tid] >> 2, bnd = sm.nstart[tid+1] >> 2; for (int i = a; i < bnd; i++) sm.sNode[i] = (unsigned char)tid; }
    __syncthreads();
#pragma unroll
    for (int k = 0; k < 2; k++) if (val[k]) {
      int a = sm.nstart[dl[k]], bnd = a + sm.ncount[dl[k]], rank = 0; u32 ev = I[k].y;
      for (int i = a; i < bnd; i++) rank += (tmpE[i] < ev) ? 1 : 0;
      int p = a + rank;
      sm.sA[p] = make_uint4(F[k].x, F[k].y, (F[k].z & 0xFFFFu) | 0x3F800000u, 0x00003F80u);
      sm.sSrc[p] = I[k].x & 0x3FFFFFFu;
      sm.sG[0][p] = 0x3F80; sm.sG[1][p] = (u16)(F[k].z >> 16); sm.sG[2][p] = (u16)(F[k].w & 0xFFFFu); sm.sG[3][p] = (u16)(F[k].w >> 16);
    }
    __syncthreads();
    STAMP(1,2);
    const int nsteps = Tp >> 2;
    per = (nsteps + 15) >> 4;
    const int st0 = __builtin_amdgcn_readfirstlane(min(nsteps, w * per));
    const int st1 = __builtin_amdgcn_readfirstlane(min(nsteps, (w+1) * per));
    int cur = -1;
    f32x4 acc2[2];
    acc2[0] = (f32x4){0.f,0.f,0.f,0.f}; acc2[1] = (f32x4){0.f,0.f,0.f,0.f};
    auto flush = [&](int n) {
      float* pt = sm.part + (n + w) * 132;
      *(float2*)(pt + 2*l) = make_float2(acc2[0][0], acc2[1][0]);
      float px = acc2[0][1]*wxv[0] + acc2[1][1]*wxv[1];
      float py = acc2[0][2]*wxv[0] + acc2[1][2]*wxv[1];
      float pz = acc2[0][3]*wxv[0] + acc2[1][3]*wxv[1];
#pragma unroll
      for (int o = 1; o < 64; o <<= 1) { px += __shfl_xor(px, o); py += __shfl_xor(py, o); pz += __shfl_xor(pz, o); }
      if (l == 0) { pt[128] = px; pt[129] = py; pt[130] = pz; }
    };
    for (int b0 = st0; b0 < st1; b0 += NS) {
      u32 q[NS][4];
#pragma unroll
      for (int k = 0; k < NS; k++) {
        if (b0 + k < st1) {
          uint4 s4 = *(const uint4*)(sm.sSrc + (b0 + k)*4);
          q[k][0] = *(const u32*)(Qb + (size_t)s4.x*128 + 2*l);
          q[k][1] = *(const u32*)(Qb + (size_t)s4.y*128 + 2*l);
          q[k][2] = *(const u32*)(Qb + (size_t)s4.z*128 + 2*l);
          q[k][3] = *(const u32*)(Qb + (size_t)s4.w*128 + 2*l);
        }
      }
#pragma unroll
      for (int k = 0; k < NS; k++) {
        if (b0 + k < st1) {
          const int st = b0 + k;
          const int n = __builtin_amdgcn_readfirstlane((int)sm.sNode[st]);
          if (n != cur) {
            if (cur >= 0) flush(cur);
            cur = n;
            acc2[0] = (f32x4){0.f,0.f,0.f,0.f}; acc2[1] = (f32x4){0.f,0.f,0.f,0.f};
            uint2 pp = *(const uint2*)(sm.sP + n*128 + 2*l);
            W1[0].u[0] = (W1[0].u[0] & 0xFFFFu) | (pp.x << 16); W1[0].u[1] = pp.x >> 16;
            W1[1].u[0] = (W1[1].u[0] & 0xFFFFu) | (pp.y << 16); W1[1].u[1] = pp.y >> 16;
          }
          uint4 a4 = sm.sA[st*4 + (l & 3)];
          S4 Aa, Ab, A2;
          Aa.u[0] = a4.x; Aa.u[1] = a4.y; Ab.u[0] = a4.z; Ab.u[1] = a4.w;
          uint2 g2 = *(const uint2*)(&sm.sG[l & 3][st*4]);
          A2.u[0] = g2.x; A2.u[1] = g2.y;
#pragma unroll
          for (int hh = 0; hh < 2; hh++) {
            f32x4 z = __builtin_amdgcn_mfma_f32_4x4x4bf16_1k(Aa.v, W0[hh].v, (f32x4){0.f,0.f,0.f,0.f}, 0, 0, 0);
            z = __builtin_amdgcn_mfma_f32_4x4x4bf16_1k(Ab.v, W1[hh].v, z, 0, 0, 0);
#pragma unroll
            for (int e = 0; e < 4; e++) {
              float qv = hh ? __uint_as_float(q[k][e] & 0xFFFF0000u) : __uint_as_float(q[k][e] << 16);
              z[e] = silu_f(z[e] + qv);
            }
            S4 B2; B2.u[0] = pk2(z[0], z[1]); B2.u[1] = pk2(z[2], z[3]);
            acc2[hh] = __builtin_amdgcn_mfma_f32_4x4x4bf16_1k(A2.v, B2.v, acc2[hh], 0, 0, 0);
          }
        }
      }
    }
    if (cur >= 0) flush(cur);
    __syncthreads();
    STAMP(1,3);
    for (int i = tid; i < 48*32; i += K2T) {
      int n = i >> 5, c4 = (i & 31) * 4;
      float4 s = make_float4(0.f,0.f,0.f,0.f);
      if (n < nn) {
        int a = sm.nstart[n] >> 2, bnd = sm.nstart[n+1] >> 2;
        if (bnd > a) {
          int wl = a / per, wh_ = (bnd - 1) / per;
          for (int ww = wl; ww <= wh_; ww++) {
            float4 v = *(const float4*)(sm.part + (n + ww)*132 + c4);
            s.x += v.x; s.y += v.y; s.z += v.z; s.w += v.w;
          }
        }
      }
      uint2 o; o.x = pk2(s.x, s.y); o.y = pk2(s.z, s.w);
      *(uint2*)(sM + n*136 + c4) = o;
    }
    if (tid < nn*3) {
      int n = tid / 3, k = tid - n*3;
      float s = 0.f;
      int a = sm.nstart[n] >> 2, bnd = sm.nstart[n+1] >> 2;
      if (bnd > a) {
        int wl = a / per, wh_ = (bnd - 1) / per;
        for (int ww = wl; ww <= wh_; ww++) s += sm.part[(n + ww)*132 + 128 + k];
      }
      out[(size_t)N*128 + (size_t)(n0+n)*3 + k] = f2bf(bf2f(pos[(n0+n)*3 + k]) + s);
    }
  } else {
    for (int n = w; n < 48; n += 16) {
      float a0 = 0.f, a1 = 0.f, px = 0.f, py = 0.f, pz = 0.f;
      if (n < nn) {
        const int node = n0 + n;
        const u32 pu0 = sm.sP[n*128 + 2*l], pu1 = sm.sP[n*128 + 2*l + 1];
        const float p0v = bf2f((u16)(pu0 & 0xFFFF)) + bf2f((u16)(pu0 >> 16)), p1v = bf2f((u16)(pu1 & 0xFFFF)) + bf2f((u16)(pu1 >> 16));
        float w50[5], w51[5];
        for (int k = 0; k < 5; k++) { w50[k] = bf2f(we[(256+k)*128 + 2*l]); w51[k] = bf2f(we[(256+k)*128 + 2*l+1]); }
        for (int e0 = 0; e0 < E; e0 += 64) {
          int e = e0 + l;
          int d = (e < E) ? dst[e] : -1;
          unsigned long long m = __ballot(d == node);
          while (m) {
            int bit = __ffsll((long long)m) - 1; m &= m - 1;
            int ee = e0 + bit; int s = src[ee];
            float dx = rbf(bf2f(pos[node*3+0]) - bf2f(pos[s*3+0]));
            float dy = rbf(bf2f(pos[node*3+1]) - bf2f(pos[s*3+1]));
            float dz = rbf(bf2f(pos[node*3+2]) - bf2f(pos[s*3+2]));
            float d2 = rbf(rbf(dx*dx) + rbf(dy*dy) + rbf(dz*dz));
            u32 qq = *(const u32*)(Qb + (size_t)s*128 + 2*l);
            float z0 = p0v + d2*w50[0], z1 = p1v + d2*w51[0];
            for (int k = 0; k < 4; k++) { float f = bf2f(ea[(size_t)ee*4+k]); z0 += f*w50[1+k]; z1 += f*w51[1+k]; }
            z0 += bf2f((u16)(qq & 0xFFFF)); z1 += bf2f((u16)(qq >> 16));
            float m0 = rbf(silu_f(z0)), m1 = rbf(silu_f(z1));
            a0 += m0; a1 += m1;
            float cs = m0*wxv[0] + m1*wxv[1];
            px += dx*cs; py += dy*cs; pz += dz*cs;
          }
        }
        for (int o = 1; o < 64; o <<= 1) { px += __shfl_xor(px, o); py += __shfl_xor(py, o); pz += __shfl_xor(pz, o); }
        if (l == 0) {
          out[(size_t)N*128 + (size_t)node*3 + 0] = f2bf(bf2f(pos[node*3+0]) + px);
          out[(size_t)N*128 + (size_t)node*3 + 1] = f2bf(bf2f(pos[node*3+1]) + py);
          out[(size_t)N*128 + (size_t)node*3 + 2] = f2bf(bf2f(pos[node*3+2]) + pz);
        }
      }
      *(u32*)(sM + n*136 + 2*l) = pk2(a0, a1);
    }
  }
  __syncthreads();
  STAMP(1,4);
  float* sOut = sm.part;
  {
    const int t = w & 7;
    const int rt0 = (w >> 3) ? 2 : 0, nrt = (w >> 3) ? 1 : 2;
    f32x4 acc[2] = {(f32x4){0.f,0.f,0.f,0.f}, (f32x4){0.f,0.f,0.f,0.f}};
#pragma unroll
    for (int s = 0; s < 8; s++) {
      U4 B; B.u = sm.sW[(s*8 + t)*64 + l];
#pragma unroll
      for (int i = 0; i < 2; i++) {
        if (i < nrt) {
          int rl = (rt0 + i)*16 + j;
          U4 A;
          if (s < 4) A.u = *(const uint4*)(sm.sH + rl*136 + 32*s + 8*g);
          else A.u = *(const uint4*)(sM + rl*136 + 32*(s-4) + 8*g);
          acc[i] = __builtin_amdgcn_mfma_f32_16x16x32_bf16(A.b, B.b, acc[i], 0, 0, 0);
        }
      }
    }
#pragma unroll
    for (int i = 0; i < 2; i++) if (i < nrt) {
#pragma unroll
      for (int r = 0; r < 4; r++) sOut[((rt0+i)*16 + 4*g + r)*132 + 8*j + t] = acc[i][r];
    }
  }
  __syncthreads();
  STAMP(1,5);
  for (int i = tid; i < nn*16; i += K2T) {
    int n = i >> 4, c8 = (i & 15) * 8;
    U4 hv2; hv2.u = *(const uint4*)(sm.sH + n*136 + c8);
    U4 o;
#pragma unroll
    for (int k = 0; k < 8; k++) { float z = sOut[n*132 + c8 + k]; o.h[k] = f2bf(bf2f(hv2.h[k]) + silu_f(z)); }
    *(uint4*)(out + (size_t)(n0+n)*128 + c8) = o.u;
  }
  STAMP(1,6);
}

extern "C" void kernel_launch(void* const* d_in, const int* in_sizes, int n_in,
                              void* d_out, int out_size, void* d_ws, size_t ws_size,
                              hipStream_t stream) {
  const u16* h=(const u16*)d_in[0]; const u16* pos=(const u16*)d_in[1]; const u16* ea=(const u16*)d_in[2];
  const int* src=(const int*)d_in[3]; const int* dst=(const int*)d_in[4];
  const u16* we=(const u16*)d_in[5]; const u16* wx=(const u16*)d_in[6]; const u16* wh=(const u16*)d_in[7];
  int N = in_sizes[0]/128; int E = in_sizes[3];
  int nchunk = (E + CH - 1)/CH; int ngemm = (N + 127)/128; int np = (N + NB - 1)/NB;
  char* ws = (char*)d_ws; size_t o = 0;
  auto take = [&](size_t bytes){ void* p = ws + o; o += (bytes + 255) & ~(size_t)255; return p; };
  u32* P = (u32*)take((size_t)N*128*4);
  u16* Qb = (u16*)take((size_t)N*128*2);
  uint4* entF = (uint4*)take((size_t)np*nchunk*CC*16);
  uint2* entI = (uint2*)take((size_t)np*nchunk*CC*8);
  u32* cntT = (u32*)take((size_t)np*nchunk*4);
  uint4* whF = (uint4*)take(65536);
  k1_kernel<<<nchunk + ngemm + 8, K1T, 0, stream>>>(h,pos,ea,src,dst,we,wh,P,Qb,entF,entI,cntT,whF,N,E,nchunk,ngemm,np
);
  k2_kernel<<<np, K2T, 0, stream>>>(h,pos,ea,src,dst,we,wx,P,Qb,entF,entI,cntT,whF,(u16*)d_out,N,E,nchunk
);
}
```

```cpp
#include <hip/hip_runtime.h>
#include <stdint.h>

typedef unsigned short u16;
typedef unsigned int u32;
typedef __bf16 bf16x8 __attribute__((ext_vector_type(8)));
typedef short s16x4 __attribute__((ext_vector_type(4)));
typedef float f32x4 __attribute__((ext_vector_type(4)));

constexpr int NB   = 40;
constexpr int CH   = 2048;
constexpr int K1T  = 512;
constexpr int K2T  = 1024;
constexpr int CAP  = 1024;
#define NSV 12
constexpr int NS   = NSV;
constexpr int NPMAX = 8192;

__device__ __forceinline__ float bf2f(u16 v){ return __uint_as_float(((u32)v)<<16); }
typedef __bf16 bf16x2 __attribute__((ext_vector_type(2)));
typedef float f32x2 __attribute__((ext_vector_type(2)));
__device__ __forceinline__ u32 pk2(float a, float b){
  f32x2 v = {a, b};
  bf16x2 r = __builtin_convertvector(v, bf16x2);
  return __builtin_bit_cast(u32, r);
}
__device__ __forceinline__ u16 f2bf(float f){ return (u16)(pk2(f, 0.f) & 0xFFFFu); }
__device__ __forceinline__ float rbf(float f){ return __uint_as_float(pk2(0.f, f) & 0xFFFF0000u); }
__device__ __forceinline__ float silu_f(float z){ return z * __builtin_amdgcn_rcpf(1.0f + __builtin_amdgcn_exp2f(-1.4426950408889634f * z)); }

template<int CTRL> __device__ __forceinline__ float dpp_add(float v){
  int x = __builtin_amdgcn_update_dpp(0, __float_as_int(v), CTRL, 0xF, 0xF, true);
  return v + __int_as_float(x);
}
__device__ __forceinline__ float wave_sum63(float v){
  v = dpp_add<0x111>(v);
  v = dpp_add<0x112>(v);
  v = dpp_add<0x114>(v);
  v = dpp_add<0x118>(v);
  v = dpp_add<0x142>(v);
  v = dpp_add<0x143>(v);
  return v;
}
struct __attribute__((packed, aligned(2))) Pk8 { unsigned long long v; };
__device__ __forceinline__ unsigned long long pos3(const u16* __restrict__ pos, int n, int N){
  const bool last = (n == N - 1);
  const Pk8* p = (const Pk8*)(pos + (size_t)n*3 - (last ? 1 : 0));
  unsigned long long v = p->v;
  return last ? (v >> 16) : v;
}

union U4 { uint4 u; bf16x8 b; u16 h[8]; };
union S4 { u32 u[2]; s16x4 v; u16 h[4]; };

#define STAMP(k, idx)
#define DSTAMP(idx)
struct K1Part { u32 hist[NPMAX]; uint4 stA[CH]; uint2 stB[CH]; u32 wtot[16]; };

__global__ __launch_bounds__(K1T) void k1_kernel(const u16* __restrict__ h, const u16* __restrict__ pos,
    const u16* __restrict__ ea, const int* __restrict__ src, const int* __restrict__ dst,
    const u16* __restrict__ we, const u16* __restrict__ wh,
    u16* __restrict__ Qb, uint4* __restrict__ entA, uint2* __restrict__ entB,
    u32* __restrict__ offT, uint4* __restrict__ wF, int N, int E, int nchunk, int ngemm, int np
)
{
  STAMP(0,0);
  __shared__ __attribute__((aligned(16))) char smem[sizeof(K1Part)];
  const int tid = threadIdx.x;
  const int b = blockIdx.x;
  if (b < nchunk) {
    K1Part& sp = *(K1Part*)smem;
    for (int i = tid; i < np; i += K1T) sp.hist[i] = 0;
    const int base = b * CH;
    int dv[4], sv[4]; uint2 av[4];
#pragma unroll
    for (int k = 0; k < 4; k++) {
      int e = min(base + k*K1T + tid, E-1);
      dv[k] = dst[e]; sv[k] = src[e]; av[k] = *(const uint2*)(ea + (size_t)e*4);
    }
    unsigned long long pd_[4], ps_[4];
#pragma unroll
    for (int k = 0; k < 4; k++) { pd_[k] = pos3(pos, dv[k], N); ps_[k] = pos3(pos, sv[k], N); }
    __syncthreads();
    STAMP(0,1);
    u32 slot[4]; int bn[4];
#pragma unroll
    for (int k = 0; k < 4; k++) {
      int e = base + k*K1T + tid;
      bn[k] = dv[k] / NB; slot[k] = 0;
      if (e < E) slot[k] = atomicAdd(&sp.hist[bn[k]], 1u);
    }
    __syncthreads();
    const int per = (np + K1T - 1) / K1T;
    const int i0 = min(tid * per, np), i1 = min(i0 + per, np);
    u32 loc = 0;
    for (int i = i0; i < i1; i++) loc += sp.hist[i];
    u32 incl = loc;
    const int l = tid & 63, w = tid >> 6;
#pragma unroll
    for (int o = 1; o < 64; o <<= 1) { u32 v = __shfl_up(incl, o); if (l >= o) incl += v; }
    if (l == 63) sp.wtot[w] = incl;
    __syncthreads();
    u32 run = incl - loc;
    for (int ww = 0; ww < w; ww++) run += sp.wtot[ww];
    for (int i = i0; i < i1; i++) { u32 c = sp.hist[i]; sp.hist[i] = run; offT[(size_t)i * nchunk + b] = run | (c << 16); run += c; }
    __syncthreads();
    STAMP(0,2);
#pragma unroll
    for (int k = 0; k < 4; k++) {
      int e = base + k*K1T + tid;
      if (e < E) {
        float dx = rbf(bf2f((u16)pd_[k]) - bf2f((u16)ps_[k]));
        float dy = rbf(bf2f((u16)(pd_[k] >> 16)) - bf2f((u16)(ps_[k] >> 16)));
        float dz = rbf(bf2f((u16)(pd_[k] >> 32)) - bf2f((u16)(ps_[k] >> 32)));
        float d2 = rbf(dx*dx) + rbf(dy*dy) + rbf(dz*dz);
        u32 dxy = pk2(dx, dy), dzd = pk2(dz, d2);
        int idx = sp.hist[bn[k]] + slot[k];
        sp.stA[idx] = make_uint4((dzd >> 16) | (av[k].x << 16), (av[k].x >> 16) | (av[k].y << 16),
                                 (av[k].y >> 16) | (dxy << 16), (dxy >> 16) | (dzd << 16));
        sp.stB[idx] = make_uint2((u32)sv[k] | ((u32)(dv[k] - bn[k]*NB) << 26), (u32)e);
      }
    }
    __syncthreads();
    const int ne = min(CH, E - base);
#pragma unroll
    for (int k = 0; k < 4; k++) { int i = tid + k*K1T; if (i < ne) entA[(size_t)base + i] = sp.stA[i]; }
#pragma unroll
    for (int k = 0; k < 2; k++) { int i = tid + k*K1T; if (2*i < ne) ((uint4*)entB)[((size_t)base >> 1) + i] = ((const uint4*)sp.stB)[i]; }
    STAMP(0,3);
  } else if (b < nchunk + ngemm) {
    u32* Wt32 = (u32*)smem;
    const int w = tid >> 6, l = tid & 63, g = l >> 4, j = l & 15;
    const int rowbase = (b - nchunk) * 128 + w * 16;
    U4 A[4];
    {
      int row = min(rowbase + j, N-1);
#pragma unroll
      for (int s = 0; s < 4; s++) A[s].u = *(const uint4*)(h + (size_t)row*128 + 32*s + 8*g);
    }
    {
      U4 r0[2], r1[2];
#pragma unroll
      for (int i4 = 0; i4 < 2; i4++) {
        int it = tid + i4*K1T;
        int oct = it & 15, kp = it >> 4;
        int k = 128 + 2*kp;
        r0[i4].u = *(const uint4*)(we + k*128 + oct*8);
        r1[i4].u = *(const uint4*)(we + (k+1)*128 + oct*8);
      }
#pragma unroll
      for (int i4 = 0; i4 < 2; i4++) {
        int it = tid + i4*K1T;
        int oct = it & 15, kp = it >> 4;
#pragma unroll
        for (int i = 0; i < 8; i++)
          Wt32[(i*16 + oct)*68 + kp] = (u32)r0[i4].h[i] | ((u32)r1[i4].h[i] << 16);
      }
    }
    __syncthreads();
    STAMP(0,1);
    f32x4 acc[8];
#pragma unroll
    for (int t = 0; t < 8; t++) acc[t] = (f32x4){0.f,0.f,0.f,0.f};
#pragma unroll
    for (int s = 0; s < 4; s++) {
#pragma unroll
      for (int t = 0; t < 8; t++) {
        U4 B; B.u = *(const uint4*)(Wt32 + (t*16 + j)*68 + 16*s + 4*g);
        acc[t] = __builtin_amdgcn_mfma_f32_16x16x32_bf16(A[s].b, B.b, acc[t], 0, 0, 0);
      }
    }
    STAMP(0,2);
#pragma unroll
    for (int r = 0; r < 4; r++) {
      int row = rowbase + 4*g + r;
      if (row < N) {
        uint4 v;
        v.x = pk2(acc[0][r], acc[1][r]); v.y = pk2(acc[2][r], acc[3][r]);
        v.z = pk2(acc[4][r], acc[5][r]); v.w = pk2(acc[6][r], acc[7][r]);
        *(uint4*)(Qb + (size_t)row*128 + 8*j) = v;
      }
    }
    STAMP(0,3);
  } else {
    int it = (b - nchunk - ngemm) * K1T + tid;
    int l = it & 63, f = (it >> 6) & 31, part = it >> 11, t = f & 7, s = f >> 3, g = l >> 4, j = l & 15;
    const u16* wsrc = (part == 0) ? (we + 16*t + j) : (wh + (part - 1)*128*128 + 8*j + t);
    U4 v;
#pragma unroll
    for (int jj = 0; jj < 8; jj++) v.h[jj] = wsrc[(32*s + 8*g + jj)*128];
    wF[it] = v.u;
    STAMP(0,3);
  }
}

struct K2Smem {
  uint4 sA[CAP];
  uint4 sW[4096];
  u32   sP[48*128];
  float part[(NB+16)*132];
  uint4 sH[48*16];
  u16   sG[4][CAP];
  u32   sSrc[CAP];
  int   ncount[64];
  int   nstart[72];
  int   flag[4];
  unsigned char sNode[CAP/4];
};

__global__ __launch_bounds__(K2T) void k2_kernel(const u16* __restrict__ h, const u16* __restrict__ pos,
    const u16* __restrict__ ea, const int* __restrict__ src, const int* __restrict__ dst,
    const u16* __restrict__ we, const u16* __restrict__ wx,
    const u16* __restrict__ Qb,
    const uint4* __restrict__ entA, const uint2* __restrict__ entB, const u32* __restrict__ offT,
    const uint4* __restrict__ wF,
    u16* __restrict__ out, int N, int E, int nchunk, int np
)
{
  DSTAMP(0);
  __shared__ __attribute__((aligned(16))) K2Smem sm;
  const int tid = threadIdx.x, w = tid >> 6, l = tid & 63, g = l >> 4, j = l & 15;
  const int blk = blockIdx.x;
  const int n0 = blk * NB;
  const int nn = min(NB, N - n0);
  u16* const sM = (u16*)sm.sA;
  u32* const tmpE = (u32*)sm.part;
  uint4* const uA = (uint4*)(sm.part + 1024);
  u32* const uS = (u32*)(sm.part + 1024 + 4096);
  const int tt = w & 7;
  const int rt0 = (w >> 3) ? 2 : 0, nrt = (w >> 3) ? 1 : 2;

  u32 oc[3];
#pragma unroll
  for (int k = 0; k < 3; k++) {
    int c = (tid + k*K2T) >> 5;
    oc[k] = offT[(size_t)blk * nchunk + min(c, nchunk - 1)];
    if (c >= nchunk) oc[k] = 0u;
  }
  for (int i = tid; i < CAP; i += K2T) {
    sm.sA[i] = make_uint4(0,0,0,0); sm.sSrc[i] = 0;
    sm.sG[0][i] = 0; sm.sG[1][i] = 0; sm.sG[2][i] = 0; sm.sG[3][i] = 0;
  }
  if (tid < 64) sm.ncount[tid] = 0;
  if (tid < 4) sm.flag[tid] = 0;
  DSTAMP(10);
  uint4 IA[3]; uint2 IB[3]; bool val[3];
#pragma unroll
  for (int k = 0; k < 3; k++) {
    int i = tid + k*K2T, c = i >> 5, kk = i & 31;
    int cnt = (int)(oc[k] >> 16);
    val[k] = kk < cnt;
    size_t idx = val[k] ? ((size_t)c * CH + (oc[k] & 0xFFFFu) + kk) : (size_t)0;
    IB[k] = entB[idx]; IA[k] = entA[idx];
  }
  const bool ovf = (nchunk > 96) || (oc[0] >> 16) > 32u || (oc[1] >> 16) > 32u || (oc[2] >> 16) > 32u;
#pragma unroll
  for (int k = 0; k < 4; k++)
    __builtin_amdgcn_global_load_lds((const unsigned*)(wF + k*K2T + tid), (unsigned*)(sm.sW + k*K2T + w*64), 16, 0, 0);
  if (w < 12) {
    const int r = w*4 + g;
    const int row = min(n0 + r, N - 1);
    __builtin_amdgcn_global_load_lds((const unsigned*)(h + (size_t)row*128 + (j ^ (r & 15))*8), (unsigned*)(sm.sH + w*64), 16, 0, 0);
  }
  __syncthreads();
  DSTAMP(1);
  int dl[3], slot[3];
  if (ovf) sm.flag[0] = 1;
#pragma unroll
  for (int k = 0; k < 3; k++) {
    dl[k] = val[k] ? (int)(IB[k].x >> 26) : 0; slot[k] = 0;
    if (val[k]) slot[k] = atomicAdd(&sm.ncount[dl[k]], 1);
  }
  const u16 bpos = pos[(size_t)n0*3 + min(tid, nn*3 - 1)];
  S4 W0[2], W1[2]; float wxv[2];
#pragma unroll
  for (int hh = 0; hh < 2; hh++) {
    int c = 2*l + hh;
#pragma unroll
    for (int k = 0; k < 4; k++) W0[hh].h[k] = we[(256+k)*128 + c];
    W1[hh].u[0] = we[260*128 + c]; W1[hh].u[1] = 0;
    wxv[hh] = bf2f(wx[c]);
  }
  __syncthreads();
  DSTAMP(7);
  if (w == 0) {
    int c = (l < NB) ? sm.ncount[l] : 0;
    int cp = (c + 3) & ~3, incl = cp;
    for (int o = 1; o < 64; o <<= 1) { int v = __shfl_up(incl, o); if (l >= o) incl += v; }
    sm.nstart[l] = incl - cp;
    if (l == 63) { sm.nstart[64] = incl; if (incl > CAP) sm.flag[0] = 1; }
  }
  __syncthreads();
  DSTAMP(8);
  const bool fast = (sm.flag[0] == 0);
  const int Tp = sm.nstart[64];
  int per = 0;
  if (fast) {
#pragma unroll
    for (int k = 0; k < 3; k++) {
      if (val[k]) {
        int p = sm.nstart[dl[k]] + slot[k];
        tmpE[p] = IB[k].y;
        *(uint4*)&uA[p] = make_uint4(IA[k].x, IA[k].y, IA[k].z, IA[k].w);
        uS[p] = IB[k].x & 0x3FFFFFFu;
      }
    }
    if (tid < NB) {
      int a = sm.nstart[tid], bnd = sm.nstart[tid+1];
      for (int i = a >> 2; i < (bnd >> 2); i++) sm.sNode[i] = (unsigned char)tid;
      for (int i = a + sm.ncount[tid]; i < bnd; i++) tmpE[i] = 0xFFFFFFFFu;
    }
    __syncthreads();
    DSTAMP(9);
    if (tid < Tp) {
      const u32 ev = tmpE[tid];
      if (ev != 0xFFFFFFFFu) {
        const int n = sm.sNode[tid >> 2];
        const int a = sm.nstart[n], bnd = sm.nstart[n+1];
        const uint4 pa = uA[tid]; const u32 ps = uS[tid];
        int rank = 0;
        for (int i = a; i < bnd; i += 4) {
          uint4 t = *(const uint4*)(tmpE + i);
          rank += (t.x < ev) + (t.y < ev) + (t.z < ev) + (t.w < ev);
        }
        const int p = a + rank;
        sm.sA[p] = make_uint4(pa.x, pa.y, (pa.z & 0xFFFFu) | 0x3F800000u, 0x00003F80u);
        sm.sSrc[p] = ps;
        sm.sG[0][p] = 0x3F80; sm.sG[1][p] = (u16)(pa.z >> 16); sm.sG[2][p] = (u16)(pa.w & 0xFFFFu); sm.sG[3][p] = (u16)(pa.w >> 16);
      }
    }
  }
  DSTAMP(11);
  f32x4 acc1[2];
#pragma unroll
  for (int i = 0; i < 2; i++) {
    acc1[i] = (f32x4){0.f,0.f,0.f,0.f};
    if (i < nrt) {
      f32x4 ap = (f32x4){0.f,0.f,0.f,0.f};
      const int r = (rt0 + i)*16 + j;
#pragma unroll
      for (int s = 0; s < 4; s++) {
        U4 A, Be, Bh;
        A.u = sm.sH[r*16 + ((4*s + g) ^ j)];
        Be.u = sm.sW[(s*8 + tt)*64 + l];
        Bh.u = sm.sW[2048 + (s*8 + tt)*64 + l];
        ap = __builtin_amdgcn_mfma_f32_16x16x32_bf16(A.b, Be.b, ap, 0, 0, 0);
        acc1[i] = __builtin_amdgcn_mfma_f32_16x16x32_bf16(A.b, Bh.b, acc1[i], 0, 0, 0);
      }
#pragma unroll
      for (int rr = 0; rr < 4; rr++) {
        float v = ap[rr]; u32 hi = pk2(0.f, v); float lo = v - __uint_as_float(hi & 0xFFFF0000u);
        sm.sP[((rt0+i)*16 + 4*g + rr)*128 + 16*tt + j] = (hi >> 16) | (pk2(0.f, lo) & 0xFFFF0000u);
      }
    }
  }
  __syncthreads();
  DSTAMP(2);
  U4 Bm[4];
  if (fast) {
    const int nsteps = Tp >> 2;
    per = (nsteps + 15) >> 4;
    const int st0 = __builtin_amdgcn_readfirstlane(min(nsteps, w * per));
    const int st1 = __builtin_amdgcn_readfirstlane(min(nsteps, (w+1) * per));
    int cur = -1;
    u32 sello = 0x00003F80u, selhi = 0x3F800000u;
    asm volatile("" : "+v"(sello), "+v"(selhi));
    f32x4 acc2[2];
    acc2[0] = (f32x4){0.f,0.f,0.f,0.f}; acc2[1] = (f32x4){0.f,0.f,0.f,0.f};
    auto flush = [&](int n) {
      float* pt = sm.part + (n + w) * 132;
      *(float2*)(pt + 2*l) = make_float2(acc2[0][0], acc2[1][0]);
      float px = wave_sum63(acc2[0][1]*wxv[0] + acc2[1][1]*wxv[1]);
      float py = wave_sum63(acc2[0][2]*wxv[0] + acc2[1][2]*wxv[1]);
      float pz = wave_sum63(acc2[0][3]*wxv[0] + acc2[1][3]*wxv[1]);
      if (l == 63) { pt[128] = px; pt[129] = py; pt[130] = pz; }
    };
    const int ndv = (int)sm.sNode[min(st0 + l, CAP/4 - 1)];
    const int ndp = (int)sm.sNode[min(max(st0 + l - 1, 0), CAP/4 - 1)];
    const unsigned long long chg = __ballot(l == 0 || ndv != ndp);
    uint4 a4n = sm.sA[st0*4 + (l & 3)];
    uint2 g2n = *(const uint2*)(&sm.sG[l & 3][st0*4]);
    for (int b0 = st0; b0 < st1; b0 += NS) {
      u32 q[NS][4];
#pragma unroll
      for (int k = 0; k < NS; k++) {
        if (b0 + k < st1) {
          uint4 s4 = *(const uint4*)(sm.sSrc + (b0 + k)*4);
          q[k][0] = *(const u32*)(Qb + (size_t)s4.x*128 + 2*l);
          q[k][1] = *(const u32*)(Qb + (size_t)s4.y*128 + 2*l);
          q[k][2] = *(const u32*)(Qb + (size_t)s4.z*128 + 2*l);
          q[k][3] = *(const u32*)(Qb + (size_t)s4.w*128 + 2*l);
        }
      }
#pragma unroll
      for (int k = 0; k < NS; k++) {
        if (b0 + k < st1) {
          const int st = b0 + k;
          const uint4 a4 = a4n; const uint2 g2 = g2n;
          {
            const int sn = min(st + 1, CAP/4 - 1);
            a4n = sm.sA[sn*4 + (l & 3)];
            g2n = *(const uint2*)(&sm.sG[l & 3][sn*4]);
          }
          if ((chg >> (st - st0)) & 1ull) {
            if (cur >= 0) flush(cur);
            const int n = __builtin_amdgcn_readlane(ndv, st - st0);
            cur = n;
            acc2[0] = (f32x4){0.f,0.f,0.f,0.f}; acc2[1] = (f32x4){0.f,0.f,0.f,0.f};
            uint2 pp = *(const uint2*)(sm.sP + n*128 + 2*l);
            W1[0].u[0] = (W1[0].u[0] & 0xFFFFu) | (pp.x << 16); W1[0].u[1] = pp.x >> 16;
            W1[1].u[0] = (W1[1].u[0] & 0xFFFFu) | (pp.y << 16); W1[1].u[1] = pp.y >> 16;
          }
          S4 Aa, Ab, A2;
          Aa.u[0] = a4.x; Aa.u[1] = a4.y; Ab.u[0] = a4.z; Ab.u[1] = a4.w;
          A2.u[0] = g2.x; A2.u[1] = g2.y;
#pragma unroll
          for (int hh = 0; hh < 2; hh++) {
            f32x4 z = __builtin_amdgcn_mfma_f32_4x4x4bf16_1k(Aa.v, W0[hh].v, (f32x4){0.f,0.f,0.f,0.f}, 0, 0, 0);
            z = __builtin_amdgcn_mfma_f32_4x4x4bf16_1k(Ab.v, W1[hh].v, z, 0, 0, 0);
#pragma unroll
            for (int e = 0; e < 4; e++) {
              float zz = __builtin_amdgcn_fdot2_f32_bf16(__builtin_bit_cast(bf16x2, q[k][e]), __builtin_bit_cast(bf16x2, hh ? selhi : sello), z[e], false);
              z[e] = silu_f(zz);
            }
            S4 B2; B2.u[0] = pk2(z[0], z[1]); B2.u[1] = pk2(z[2], z[3]);
            acc2[hh] = __builtin_amdgcn_mfma_f32_4x4x4bf16_1k(A2.v, B2.v, acc2[hh], 0, 0, 0);
          }
        }
      }
    }
#pragma unroll
    for (int s = 0; s < 4; s++) Bm[s].u = wF[4096 + (s*8 + tt)*64 + l];
    if (cur >= 0) flush(cur);
    __syncthreads();
    DSTAMP(3);
    for (int i = tid; i < 48*32; i += K2T) {
      int n = i >> 5, c4 = (i & 31) * 4;
      float4 s = make_float4(0.f,0.f,0.f,0.f);
      if (n < nn) {
        int a = sm.nstart[n] >> 2, bnd = sm.nstart[n+1] >> 2;
        if (bnd > a) {
          int wl = a / per, wh_ = (bnd - 1) / per;
          for (int ww = wl; ww <= wh_; ww++) {
            float4 v = *(const float4*)(sm.part + (n + ww)*132 + c4);
            s.x += v.x; s.y += v.y; s.z += v.z; s.w += v.w;
          }
        }
      }
      uint2 o; o.x = pk2(s.x, s.y); o.y = pk2(s.z, s.w);
      *(uint2*)(sM + n*136 + c4) = o;
    }
    if (tid < nn*3) {
      int n = tid / 3, k = tid - n*3;
      float s = 0.f;
      int a = sm.nstart[n] >> 2, bnd = sm.nstart[n+1] >> 2;
      if (bnd > a) {
        int wl = a / per, wh_ = (bnd - 1) / per;
        for (int ww = wl; ww <= wh_; ww++) s += sm.part[(n + ww)*132 + 128 + k];
      }
      out[(size_t)N*128 + (size_t)(n0+n)*3 + k] = f2bf(bf2f(bpos) + s);
    }
  } else {
#pragma unroll
    for (int s = 0; s < 4; s++) Bm[s].u = wF[4096 + (s*8 + tt)*64 + l];
    for (int n = w; n < 48; n += 16) {
      float a0 = 0.f, a1 = 0.f, px = 0.f, py = 0.f, pz = 0.f;
      if (n < nn) {
        const int node = n0 + n;
        const u32 pu0 = sm.sP[n*128 + 2*l], pu1 = sm.sP[n*128 + 2*l + 1];
        const float p0v = bf2f((u16)(pu0 & 0xFFFF)) + bf2f((u16)(pu0 >> 16)), p1v = bf2f((u16)(pu1 & 0xFFFF)) + bf2f((u16)(pu1 >> 16));
        float w50[5], w51[5];
        for (int k = 0; k < 5; k++) { w50[k] = bf2f(we[(256+k)*128 + 2*l]); w51[k] = bf2f(we[(256+k)*128 + 2*l+1]); }
        for (int e0 = 0; e0 < E; e0 += 64) {
          int e = e0 + l;
          int d = (e < E) ? dst[e] : -1;
          unsigned long long m = __ballot(d == node);
          while (m) {
            int bit = __ffsll((long long)m) - 1; m &= m - 1;
            int ee = e0 + bit; int s = src[ee];
            float dx = rbf(bf2f(pos[node*3+0]) - bf2f(pos[s*3+0]));
            float dy = rbf(bf2f(pos[node*3+1]) - bf2f(pos[s*3+1]));
            float dz = rbf(bf2f(pos[node*3+2]) - bf2f(pos[s*3+2]));
            float d2 = rbf(rbf(dx*dx) + rbf(dy*dy) + rbf(dz*dz));
            u32 qq = *(const u32*)(Qb + (size_t)s*128 + 2*l);
            float z0 = p0v + d2*w50[0], z1 = p1v + d2*w51[0];
            for (int k = 0; k < 4; k++) { float f = bf2f(ea[(size_t)ee*4+k]); z0 += f*w50[1+k]; z1 += f*w51[1+k]; }
            z0 += bf2f((u16)(qq & 0xFFFF)); z1 += bf2f((u16)(qq >> 16));
            float m0 = rbf(silu_f(z0)), m1 = rbf(silu_f(z1));
            a0 += m0; a1 += m1;
            float cs = m0*wxv[0] + m1*wxv[1];
            px += dx*cs; py += dy*cs; pz += dz*cs;
          }
        }
        for (int o = 1; o < 64; o <<= 1) { px += __shfl_xor(px, o); py += __shfl_xor(py, o); pz += __shfl_xor(pz, o); }
        if (l == 0) {
          out[(size_t)N*128 + (size_t)node*3 + 0] = f2bf(bf2f(pos[node*3+0]) + px);
          out[(size_t)N*128 + (size_t)node*3 + 1] = f2bf(bf2f(pos[node*3+1]) + py);
          out[(size_t)N*128 + (size_t)node*3 + 2] = f2bf(bf2f(pos[node*3+2]) + pz);
        }
      }
      *(u32*)(sM + n*136 + 2*l) = pk2(a0, a1);
    }
  }
  __syncthreads();
  DSTAMP(4);
  float* sOut = sm.part;
  {
#pragma unroll
    for (int s = 0; s < 4; s++) {
#pragma unroll
      for (int i = 0; i < 2; i++) {
        if (i < nrt) {
          int rl = (rt0 + i)*16 + j;
          U4 A; A.u = *(const uint4*)(sM + rl*136 + 32*s + 8*g);
          acc1[i] = __builtin_amdgcn_mfma_f32_16x16x32_bf16(A.b, Bm[s].b, acc1[i], 0, 0, 0);
        }
      }
    }
#pragma unroll
    for (int i = 0; i < 2; i++) if (i < nrt) {
#pragma unroll
      for (int r = 0; r < 4; r++) sOut[((rt0+i)*16 + 4*g + r)*132 + 8*j + tt] = acc1[i][r];
    }
  }
  __syncthreads();
  DSTAMP(5);
  for (int i = tid; i < nn*16; i += K2T) {
    int n = i >> 4, c8 = (i & 15) * 8;
    U4 hv2; hv2.u = sm.sH[n*16 + ((i & 15) ^ (n & 15))];
    U4 o;
#pragma unroll
    for (int k = 0; k < 8; k++) { float z = sOut[n*132 + c8 + k]; o.h[k] = f2bf(bf2f(hv2.h[k]) + silu_f(z)); }
    *(uint4*)(out + (size_t)(n0+n)*128 + c8) = o.u;
  }
  DSTAMP(6);
}

extern "C" void kernel_launch(void* const* d_in, const int* in_sizes, int n_in,
                              void* d_out, int out_size, void* d_ws, size_t ws_size,
                              hipStream_t stream) {
  const u16* h=(const u16*)d_in[0]; const u16* pos=(const u16*)d_in[1]; const u16* ea=(const u16*)d_in[2];
  const int* src=(const int*)d_in[3]; const int* dst=(const int*)d_in[4];
  const u16* we=(const u16*)d_in[5]; const u16* wx=(const u16*)d_in[6]; const u16* wh=(const u16*)d_in[7];
  int N = in_sizes[0]/128; int E = in_sizes[3];
  int nchunk = (E + CH - 1)/CH; int ngemm = (N + 127)/128; int np = (N + NB - 1)/NB;
  char* ws = (char*)d_ws; size_t o = 0;
  auto take = [&](size_t bytes){ void* p = ws + o; o += (bytes + 255) & ~(size_t)255; return p; };
  u16* Qb = (u16*)take((size_t)N*128*2);
  uint4* entA = (uint4*)take((size_t)nchunk*CH*16);
  uint2* entB = (uint2*)take((size_t)nchunk*CH*8);
  u32* offT = (u32*)take((size_t)np*nchunk*4);
  uint4* wF = (uint4*)take(98304);
  k1_kernel<<<nchunk + ngemm + 12, K1T, 0, stream>>>(h,pos,ea,src,dst,we,wh,Qb,entA,entB,offT,wF,N,E,nchunk,ngemm,np
);
  k2_kernel<<<np, K2T, 0, stream>>>(h,pos,ea,src,dst,we,wx,Qb,entA,entB,offT,wF,(u16*)d_out,N,E,nchunk,np
);
}
```

```cpp
#include <hip/hip_runtime.h>
#include <stdint.h>

typedef unsigned short u16;
typedef unsigned int u32;
typedef __bf16 bf16x8 __attribute__((ext_vector_type(8)));
typedef short s16x4 __attribute__((ext_vector_type(4)));
typedef float f32x4 __attribute__((ext_vector_type(4)));

constexpr int NB   = 40;
constexpr int CH   = 2048;
constexpr int K1T  = 512;
constexpr int K2T  = 1024;
constexpr int CAP  = 1024;
#define NSV 8
constexpr int NS   = NSV;
constexpr int NPMAX = 8192;

__device__ __forceinline__ float bf2f(u16 v){ return __uint_as_float(((u32)v)<<16); }
typedef __bf16 bf16x2 __attribute__((ext_vector_type(2)));
typedef float f32x2 __attribute__((ext_vector_type(2)));
__device__ __forceinline__ u32 pk2(float a, float b){
  f32x2 v = {a, b};
  bf16x2 r = __builtin_convertvector(v, bf16x2);
  return __builtin_bit_cast(u32, r);
}
__device__ __forceinline__ u16 f2bf(float f){ return (u16)(pk2(f, 0.f) & 0xFFFFu); }
__device__ __forceinline__ float rbf(float f){ return __uint_as_float(pk2(0.f, f) & 0xFFFF0000u); }
__device__ __forceinline__ float silu_f(float z){ return z * __builtin_amdgcn_rcpf(1.0f + __builtin_amdgcn_exp2f(-1.4426950408889634f * z)); }

template<int CTRL> __device__ __forceinline__ float dpp_add(float v){
  int x = __builtin_amdgcn_update_dpp(0, __float_as_int(v), CTRL, 0xF, 0xF, true);
  return v + __int_as_float(x);
}
__device__ __forceinline__ float wave_sum63(float v){
  v = dpp_add<0x111>(v);
  v = dpp_add<0x112>(v);
  v = dpp_add<0x114>(v);
  v = dpp_add<0x118>(v);
  v = dpp_add<0x142>(v);
  v = dpp_add<0x143>(v);
  return v;
}
struct __attribute__((packed, aligned(2))) Pk8 { unsigned long long v; };
__device__ __forceinline__ unsigned long long pos3(const u16* __restrict__ pos, int n, int N){
  const bool last = (n == N - 1);
  const Pk8* p = (const Pk8*)(pos + (size_t)n*3 - (last ? 1 : 0));
  unsigned long long v = p->v;
  return last ? (v >> 16) : v;
}

union U4 { uint4 u; bf16x8 b; u16 h[8]; };
union S4 { u32 u[2]; s16x4 v; u16 h[4]; };

#define STAMP(k, idx)
#define DSTAMP(idx)
struct K1Part { u32 hist[NPMAX]; uint4 stA[CH]; u32 wtot[16]; };

__global__ __launch_bounds__(K1T) void k1_kernel(const u16* __restrict__ h,
    const u16* __restrict__ ea, const int* __restrict__ src, const int* __restrict__ dst,
    const u16* __restrict__ we, const u16* __restrict__ wh,
    u16* __restrict__ Qb, uint4* __restrict__ entA,
    u32* __restrict__ offT, uint4* __restrict__ wF, int N, int E, int nchunk, int ngemm, int np
)
{
  STAMP(0,0);
  __shared__ __attribute__((aligned(16))) char smem[sizeof(K1Part)];
  const int tid = threadIdx.x;
  const int b = blockIdx.x;
  if (b < nchunk) {
    K1Part& sp = *(K1Part*)smem;
    for (int i = tid; i < np; i += K1T) sp.hist[i] = 0;
    const int base = b * CH;
    int dv[4], sv[4]; uint2 av[4];
#pragma unroll
    for (int k = 0; k < 4; k++) {
      int e = min(base + k*K1T + tid, E-1);
      dv[k] = dst[e]; sv[k] = src[e]; av[k] = *(const uint2*)(ea + (size_t)e*4);
    }
    __syncthreads();
    STAMP(0,1);
    u32 slot[4]; int bn[4];
#pragma unroll
    for (int k = 0; k < 4; k++) {
      int e = base + k*K1T + tid;
      bn[k] = dv[k] / NB; slot[k] = 0;
      if (e < E) slot[k] = atomicAdd(&sp.hist[bn[k]], 1u);
    }
    __syncthreads();
    const int per = (np + K1T - 1) / K1T;
    const int i0 = min(tid * per, np), i1 = min(i0 + per, np);
    u32 loc = 0;
    for (int i = i0; i < i1; i++) loc += sp.hist[i];
    u32 incl = loc;
    const int l = tid & 63, w = tid >> 6;
#pragma unroll
    for (int o = 1; o < 64; o <<= 1) { u32 v = __shfl_up(incl, o); if (l >= o) incl += v; }
    if (l == 63) sp.wtot[w] = incl;
    __syncthreads();
    u32 run = incl - loc;
    for (int ww = 0; ww < w; ww++) run += sp.wtot[ww];
    for (int i = i0; i < i1; i++) { u32 c = sp.hist[i]; sp.hist[i] = run; offT[(size_t)i * nchunk + b] = run | (c << 16); run += c; }
    __syncthreads();
    STAMP(0,2);
#pragma unroll
    for (int k = 0; k < 4; k++) {
      int e = base + k*K1T + tid;
      if (e < E) {
        int idx = sp.hist[bn[k]] + slot[k];
        sp.stA[idx] = make_uint4(av[k].x, av[k].y, (u32)sv[k] | ((u32)(dv[k] - bn[k]*NB) << 26), (u32)e);
      }
    }
    __syncthreads();
    const int ne = min(CH, E - base);
#pragma unroll
    for (int k = 0; k < 4; k++) { int i = tid + k*K1T; if (i < ne) entA[(size_t)base + i] = sp.stA[i]; }
    STAMP(0,3);
  } else if (b < nchunk + ngemm) {
    u32* Wt32 = (u32*)smem;
    const int w = tid >> 6, l = tid & 63, g = l >> 4, j = l & 15;
    const int rowbase = (b - nchunk) * 128 + w * 16;
    U4 A[4];
    {
      int row = min(rowbase + j, N-1);
#pragma unroll
      for (int s = 0; s < 4; s++) A[s].u = *(const uint4*)(h + (size_t)row*128 + 32*s + 8*g);
    }
    {
      U4 r0[2], r1[2];
#pragma unroll
      for (int i4 = 0; i4 < 2; i4++) {
        int it = tid + i4*K1T;
        int oct = it & 15, kp = it >> 4;
        int k = 128 + 2*kp;
        r0[i4].u = *(const uint4*)(we + k*128 + oct*8);
        r1[i4].u = *(const uint4*)(we + (k+1)*128 + oct*8);
      }
#pragma unroll
      for (int i4 = 0; i4 < 2; i4++) {
        int it = tid + i4*K1T;
        int oct = it & 15, kp = it >> 4;
#pragma unroll
        for (int i = 0; i < 8; i++)
          Wt32[(i*16 + oct)*68 + kp] = (u32)r0[i4].h[i] | ((u32)r1[i4].h[i] << 16);
      }
    }
    __syncthreads();
    STAMP(0,1);
    f32x4 acc[8];
#pragma unroll
    for (int t = 0; t < 8; t++) acc[t] = (f32x4){0.f,0.f,0.f,0.f};
#pragma unroll
    for (int s = 0; s < 4; s++) {
#pragma unroll
      for (int t = 0; t < 8; t++) {
        U4 B; B.u = *(const uint4*)(Wt32 + (t*16 + j)*68 + 16*s + 4*g);
        acc[t] = __builtin_amdgcn_mfma_f32_16x16x32_bf16(A[s].b, B.b, acc[t], 0, 0, 0);
      }
    }
    STAMP(0,2);
#pragma unroll
    for (int r = 0; r < 4; r++) {
      int row = rowbase + 4*g + r;
      if (row < N) {
        uint4 v;
        v.x = pk2(acc[0][r], acc[1][r]); v.y = pk2(acc[2][r], acc[3][r]);
        v.z = pk2(acc[4][r], acc[5][r]); v.w = pk2(acc[6][r], acc[7][r]);
        *(uint4*)(Qb + (size_t)row*128 + 8*j) = v;
      }
    }
    STAMP(0,3);
  } else {
    int it = (b - nchunk - ngemm) * K1T + tid;
    int l = it & 63, f = (it >> 6) & 31, part = it >> 11, t = f & 7, s = f >> 3, g = l >> 4, j = l & 15;
    const u16* wsrc = (part == 0) ? (we + 16*t + j) : (wh + (part - 1)*128*128 + 8*j + t);
    U4 v;
#pragma unroll
    for (int jj = 0; jj < 8; jj++) v.h[jj] = wsrc[(32*s + 8*g + jj)*128];
    wF[it] = v.u;
    STAMP(0,3);
  }
}

__device__ __forceinline__ void lds_barrier(){ asm volatile("s_waitcnt lgkmcnt(0)\n\ts_barrier" ::: "memory"); }
__device__ __forceinline__ void all_barrier(){ asm volatile("s_waitcnt vmcnt(0) lgkmcnt(0)\n\ts_barrier" ::: "memory"); }

typedef __attribute__((address_space(3))) const void* lds_cptr;
__device__ __forceinline__ u32 lds_addr(const void* p){ return __builtin_amdgcn_readfirstlane((u32)(uintptr_t)(lds_cptr)p); }
__device__ __forceinline__ void dma16(const void* g, u32 lds){
  asm volatile("s_mov_b32 m0, %1\n\ts_nop 0\n\tglobal_load_lds_dwordx4 %0, off" :: "v"(g), "s"(lds) : "memory");
}
__device__ __forceinline__ void dma4(const void* g, u32 lds){
  asm volatile("s_mov_b32 m0, %1\n\ts_nop 0\n\tglobal_load_lds_dword %0, off" :: "v"(g), "s"(lds) : "memory");
}

struct K2Smem {
  uint4 sA[CAP];
  uint4 sW[4096];
  u32   sP[48*128];
  float part[(NB+16)*132];
  uint4 sH[48*16];
  u16   sG[4][CAP];
  u32   sSrc[CAP+4];
  int   ncount[64];
  int   nstart[72];
  int   flag[4];
  u32   dummy[64];
  u16   posd[128];
  unsigned char sNode[CAP/4];
};

__global__ __launch_bounds__(K2T) void k2_kernel(const u16* __restrict__ h, const u16* __restrict__ pos,
    const u16* __restrict__ ea, const int* __restrict__ src, const int* __restrict__ dst,
    const u16* __restrict__ we, const u16* __restrict__ wx,
    const u16* __restrict__ Qb,
    const uint4* __restrict__ entA, const u32* __restrict__ offT,
    const uint4* __restrict__ wF,
    u16* __restrict__ out, int N, int E, int nchunk, int np
)
{
  DSTAMP(0);
  __shared__ __attribute__((aligned(16))) K2Smem sm;
  const int tid = threadIdx.x, w = tid >> 6, l = tid & 63, g = l >> 4, j = l & 15;
  const int blk = blockIdx.x;
  const int n0 = blk * NB;
  const int nn = min(NB, N - n0);
  u16* const sM = (u16*)sm.sA;
  u32* const tmpE = (u32*)sm.part;
  uint4* const uA = (uint4*)(sm.part + 1024);
  u32* const uS = (u32*)(sm.part + 1024 + 4096);
  const int tt = w & 7;
  const int rt0 = (w >> 3) ? 2 : 0, nrt = (w >> 3) ? 1 : 2;

  u32 bposv, oc[3];
  {
    const u16* pb = pos + (size_t)n0*3 + min(tid, nn*3 - 1);
    asm volatile("global_load_ushort %0, %1, off" : "=v"(bposv) : "v"(pb) : "memory");
#pragma unroll
    for (int k = 0; k < 3; k++) {
      int c = (tid + k*K2T) >> 5;
      const u32* po = offT + (size_t)blk * nchunk + min(c, nchunk - 1);
      asm volatile("global_load_dword %0, %1, off" : "=v"(oc[k]) : "v"(po) : "memory");
    }
  }
#pragma unroll
  for (int k = 0; k < 3; k++) {
    const int id = min(w + 16*k, 43);
    if (id < 12) {
      const int r = id*4 + g;
      const int row = min(n0 + r, N - 1);
      dma16(h + (size_t)row*128 + (j ^ (r & 15))*8, lds_addr(sm.sH + id*64));
    } else {
      dma16(wF + (id - 12)*64 + l, lds_addr(sm.sW + (id - 12)*64));
    }
  }
  for (int i = tid; i < CAP; i += K2T) {
    sm.sA[i] = make_uint4(0,0,0,0); sm.sSrc[i] = 0;
    sm.sG[0][i] = 0; sm.sG[1][i] = 0; sm.sG[2][i] = 0; sm.sG[3][i] = 0;
  }
  if (tid < 64) sm.ncount[tid] = 0;
  if (tid < 4) { sm.flag[tid] = 0; sm.sSrc[CAP + tid] = 0; }
  asm volatile("s_waitcnt vmcnt(3)" : "+v"(bposv), "+v"(oc[0]), "+v"(oc[1]), "+v"(oc[2]) :: "memory");
  const u16 bpos = (u16)bposv;
#pragma unroll
  for (int k = 0; k < 3; k++) if (((tid + k*K2T) >> 5) >= nchunk) oc[k] = 0u;
  DSTAMP(10);
  uint4 IA[3]; bool val[3];
#pragma unroll
  for (int k = 0; k < 3; k++) {
    int i = tid + k*K2T, c = i >> 5, kk = i & 31;
    int cnt = (int)(oc[k] >> 16);
    val[k] = kk < cnt;
    size_t idx = val[k] ? ((size_t)c * CH + (oc[k] & 0xFFFFu) + kk) : (size_t)0;
    IA[k] = entA[idx];
  }
  const bool ovf = (nchunk > 96) || (N > (1 << 24)) || (oc[0] >> 16) > 32u || (oc[1] >> 16) > 32u || (oc[2] >> 16) > 32u;
  if (tid < 128) sm.posd[tid] = bpos;
  all_barrier();
  unsigned long long ps_[3];
#pragma unroll
  for (int k = 0; k < 3; k++) ps_[k] = pos3(pos, (int)(IA[k].z & 0x3FFFFFFu), N);
  DSTAMP(1);
  int dl[3], slot[3];
  if (ovf) sm.flag[0] = 1;
#pragma unroll
  for (int k = 0; k < 3; k++) {
    dl[k] = val[k] ? (int)(IA[k].z >> 26) : 0; slot[k] = 0;
    if (val[k]) slot[k] = atomicAdd(&sm.ncount[dl[k]], 1);
  }
  S4 W0[2], W1[2]; float wxv[2];
#pragma unroll
  for (int hh = 0; hh < 2; hh++) {
    int c = 2*l + hh;
#pragma unroll
    for (int k = 0; k < 4; k++) W0[hh].h[k] = we[(256+k)*128 + c];
    W1[hh].u[0] = 0; W1[hh].u[1] = we[260*128 + c];
    wxv[hh] = bf2f(wx[c]);
  }
#pragma unroll
  for (int i = 0; i < 2; i++) {
    if (i < nrt) {
      f32x4 ap = (f32x4){0.f,0.f,0.f,0.f};
      const int r = (rt0 + i)*16 + j;
#pragma unroll
      for (int s = 0; s < 4; s++) {
        U4 A, Be;
        A.u = sm.sH[r*16 + ((4*s + g) ^ j)];
        Be.u = sm.sW[(s*8 + tt)*64 + l];
        ap = __builtin_amdgcn_mfma_f32_16x16x32_bf16(A.b, Be.b, ap, 0, 0, 0);
      }
#pragma unroll
      for (int rr = 0; rr < 4; rr++) {
        float v = ap[rr]; u32 hi = pk2(0.f, v); float lo = v - __uint_as_float(hi & 0xFFFF0000u);
        sm.sP[((rt0+i)*16 + 4*g + rr)*128 + 16*tt + j] = (hi >> 16) | (pk2(0.f, lo) & 0xFFFF0000u);
      }
    }
  }
  lds_barrier();
  DSTAMP(7);
  int nst;
  {
    int c = (l < NB) ? sm.ncount[l] : 0;
    int cp = (c + 3) & ~3, incl = cp;
#pragma unroll
    for (int o = 1; o < 64; o <<= 1) { int v = __shfl_up(incl, o); if (l >= o) incl += v; }
    nst = incl - cp;
    if (w == 0) { sm.nstart[l] = nst; if (l == 63) sm.nstart[64] = incl; }
  }
  const int Tp = __builtin_amdgcn_readlane(nst, 63);
  const bool fast = (sm.flag[0] == 0) && (Tp <= CAP);
  DSTAMP(8);
  if (fast) {
#pragma unroll
    for (int k = 0; k < 3; k++) {
      const int p = __shfl(nst, dl[k]) + slot[k];
      if (val[k]) {
        const u16* pd = sm.posd + dl[k]*3;
        float dx = rbf(bf2f(pd[0]) - bf2f((u16)ps_[k]));
        float dy = rbf(bf2f(pd[1]) - bf2f((u16)(ps_[k] >> 16)));
        float dz = rbf(bf2f(pd[2]) - bf2f((u16)(ps_[k] >> 32)));
        float d2 = rbf(dx*dx) + rbf(dy*dy) + rbf(dz*dz);
        u32 dxy = pk2(dx, dy), dzd = pk2(dz, d2);
        tmpE[p] = IA[k].w;
        *(uint4*)&uA[p] = make_uint4((dzd >> 16) | (IA[k].x << 16), (IA[k].x >> 16) | (IA[k].y << 16),
                                     (IA[k].y >> 16) | (dxy << 16), (dxy >> 16) | (dzd << 16));
        uS[p] = (IA[k].z & 0x3FFFFFFu) << 8;
      }
    }
    if (tid < NB) {
      const int a = nst, cnt = sm.ncount[tid], bnd = a + ((cnt + 3) & ~3);
      for (int i = a >> 2; i < (bnd >> 2); i++) sm.sNode[i] = (unsigned char)tid;
      for (int i = a + cnt; i < bnd; i++) tmpE[i] = 0xFFFFFFFFu;
    }
    lds_barrier();
    DSTAMP(9);
    if (tid < Tp) {
      const u32 ev = tmpE[tid];
      if (ev != 0xFFFFFFFFu) {
        const int n = sm.sNode[tid >> 2];
        const int a = sm.nstart[n], bnd = sm.nstart[n+1];
        const uint4 pa = uA[tid]; const u32 ps = uS[tid];
        int rank = 0;
        for (int i = a; i < bnd; i += 4) {
          uint4 t = *(const uint4*)(tmpE + i);
          rank += (t.x < ev) + (t.y < ev) + (t.z < ev) + (t.w < ev);
        }
        const int p = a + rank;
        sm.sA[p] = make_uint4(pa.x, pa.y, 0x3F803F80u, pa.z & 0xFFFFu);
        sm.sSrc[p] = ps;
        sm.sG[0][p] = 0x3F80; sm.sG[1][p] = (u16)(pa.z >> 16); sm.sG[2][p] = (u16)(pa.w & 0xFFFFu); sm.sG[3][p] = (u16)(pa.w >> 16);
      }
    }
  }
  DSTAMP(11);
  all_barrier();
  DSTAMP(12);
  const int nsteps = fast ? (Tp >> 2) : 0;
  const int st0 = __builtin_amdgcn_readfirstlane((w * nsteps) >> 4);
  const int st1 = __builtin_amdgcn_readfirstlane(((w + 1) * nsteps) >> 4);
  DSTAMP(2);
  if (fast) {
    int cur = -1;
    S4 I4;
    I4.u[0] = ((l & 3) == 0) ? 0x00003F80u : (((l & 3) == 1) ? 0x3F800000u : 0u);
    I4.u[1] = ((l & 3) == 2) ? 0x00003F80u : (((l & 3) == 3) ? 0x3F800000u : 0u);
    f32x4 acc2[2];
    acc2[0] = (f32x4){0.f,0.f,0.f,0.f}; acc2[1] = (f32x4){0.f,0.f,0.f,0.f};
    auto flush = [&](int n) {
      float* pt = sm.part + (n + w) * 132;
      pt[2*l] = acc2[0][0]; pt[2*l + 1] = acc2[1][0];
      float px = acc2[0][1]*wxv[0] + acc2[1][1]*wxv[1];
      float py = acc2[0][2]*wxv[0] + acc2[1][2]*wxv[1];
      float pz = acc2[0][3]*wxv[0] + acc2[1][3]*wxv[1];
      asm volatile(
        "s_nop 1\n\t"
        "v_add_f32_dpp %0, %0, %0 row_shr:1 row_mask:0xf bank_mask:0xf bound_ctrl:1\n\t"
        "v_add_f32_dpp %1, %1, %1 row_shr:1 row_mask:0xf bank_mask:0xf bound_ctrl:1\n\t"
        "v_add_f32_dpp %2, %2, %2 row_shr:1 row_mask:0xf bank_mask:0xf bound_ctrl:1\n\t"
        "v_add_f32_dpp %0, %0, %0 row_shr:2 row_mask:0xf bank_mask:0xf bound_ctrl:1\n\t"
        "v_add_f32_dpp %1, %1, %1 row_shr:2 row_mask:0xf bank_mask:0xf bound_ctrl:1\n\t"
        "v_add_f32_dpp %2, %2, %2 row_shr:2 row_mask:0xf bank_mask:0xf bound_ctrl:1\n\t"
        "v_add_f32_dpp %0, %0, %0 row_shr:4 row_mask:0xf bank_mask:0xf bound_ctrl:1\n\t"
        "v_add_f32_dpp %1, %1, %1 row_shr:4 row_mask:0xf bank_mask:0xf bound_ctrl:1\n\t"
        "v_add_f32_dpp %2, %2, %2 row_shr:4 row_mask:0xf bank_mask:0xf bound_ctrl:1\n\t"
        "v_add_f32_dpp %0, %0, %0 row_shr:8 row_mask:0xf bank_mask:0xf bound_ctrl:1\n\t"
        "v_add_f32_dpp %1, %1, %1 row_shr:8 row_mask:0xf bank_mask:0xf bound_ctrl:1\n\t"
        "v_add_f32_dpp %2, %2, %2 row_shr:8 row_mask:0xf bank_mask:0xf bound_ctrl:1\n\t"
        "v_add_f32_dpp %0, %0, %0 row_bcast:15 row_mask:0xa bank_mask:0xf\n\t"
        "v_add_f32_dpp %1, %1, %1 row_bcast:15 row_mask:0xa bank_mask:0xf\n\t"
        "v_add_f32_dpp %2, %2, %2 row_bcast:15 row_mask:0xa bank_mask:0xf\n\t"
        "v_add_f32_dpp %0, %0, %0 row_bcast:31 row_mask:0xc bank_mask:0xf\n\t"
        "v_add_f32_dpp %1, %1, %1 row_bcast:31 row_mask:0xc bank_mask:0xf\n\t"
        "v_add_f32_dpp %2, %2, %2 row_bcast:31 row_mask:0xc bank_mask:0xf\n\t"
        : "+v"(px), "+v"(py), "+v"(pz));
      if (l == 63) { pt[128] = px; pt[129] = py; pt[130] = pz; }
    };
    const int ndv = (int)sm.sNode[min(st0 + l, CAP/4 - 1)];
    const int ndp = (int)sm.sNode[min(max(st0 + l - 1, 0), CAP/4 - 1)];
    const unsigned long long chg = __ballot(l == 0 || ndv != ndp);
    const u32 ringb = lds_addr(sm.sW + w*256);
    const u32* ringp = (const u32*)(sm.sW + w*256);
    auto qdma = [&](int st, int slot) {
      const u32 so = sm.sSrc[st*4 + (l >> 4)];
      dma16((const char*)Qb + (so + (u32)(l & 15)*16u), ringb + (u32)slot*1024u);
    };
    struct Ops { u32 q0, q1, q2, q3; uint4 a4; uint2 g2; };
    auto rdops = [&](Ops& o, int st) {
      const u32* rp = ringp + ((st - st0) & 3)*256 + l;
      o.q0 = rp[0]; o.q1 = rp[64]; o.q2 = rp[128]; o.q3 = rp[192];
      o.a4 = sm.sA[st*4 + (l & 3)];
      o.g2 = *(const uint2*)(&sm.sG[l & 3][st*4]);
    };
    auto body = [&](int st, Ops& c, Ops& nx) {
      if (st + 1 < st1) {
        if (st + 4 < st1) asm volatile("s_waitcnt vmcnt(3)" ::: "memory");
        else              asm volatile("s_waitcnt vmcnt(0)" ::: "memory");
        rdops(nx, st + 1);
      }
      const bool nw = (chg >> (st - st0)) & 1ull;
      if (nw) {
        if (cur >= 0) flush(cur);
        const int n = __builtin_amdgcn_readlane(ndv, st - st0);
        cur = n;
        W1[0].u[0] = sm.sP[n*128 + 2*l]; W1[1].u[0] = sm.sP[n*128 + 2*l + 1];
      }
      S4 B2[2];
      S4 Aa, Ab, A2;
      Aa.u[0] = c.a4.x; Aa.u[1] = c.a4.y; Ab.u[0] = c.a4.z; Ab.u[1] = c.a4.w;
      A2.u[0] = c.g2.x; A2.u[1] = c.g2.y;
#pragma unroll
      for (int hh = 0; hh < 2; hh++) {
        S4 Bq;
        Bq.u[0] = __builtin_amdgcn_perm(c.q1, c.q0, hh ? 0x07060302u : 0x05040100u);
        Bq.u[1] = __builtin_amdgcn_perm(c.q3, c.q2, hh ? 0x07060302u : 0x05040100u);
        f32x4 z = __builtin_amdgcn_mfma_f32_4x4x4bf16_1k(Aa.v, W0[hh].v, (f32x4){0.f,0.f,0.f,0.f}, 0, 0, 0);
        z = __builtin_amdgcn_mfma_f32_4x4x4bf16_1k(Ab.v, W1[hh].v, z, 0, 0, 0);
        z = __builtin_amdgcn_mfma_f32_4x4x4bf16_1k(I4.v, Bq.v, z, 0, 0, 0);
        f32x2 za = {z[0], z[1]}, zb = {z[2], z[3]};
        f32x2 ta = za * -1.4426950408889634f, tb = zb * -1.4426950408889634f;
        f32x2 xa = {__builtin_amdgcn_exp2f(ta[0]), __builtin_amdgcn_exp2f(ta[1])};
        f32x2 xb = {__builtin_amdgcn_exp2f(tb[0]), __builtin_amdgcn_exp2f(tb[1])};
        xa = xa + 1.0f; xb = xb + 1.0f;
        f32x2 ra = {__builtin_amdgcn_rcpf(xa[0]), __builtin_amdgcn_rcpf(xa[1])};
        f32x2 rb = {__builtin_amdgcn_rcpf(xb[0]), __builtin_amdgcn_rcpf(xb[1])};
        za = za * ra; zb = zb * rb;
        B2[hh].u[0] = pk2(za[0], za[1]); B2[hh].u[1] = pk2(zb[0], zb[1]);
      }
      if (nw) {
        acc2[0] = __builtin_amdgcn_mfma_f32_4x4x4bf16_1k(A2.v, B2[0].v, (f32x4){0.f,0.f,0.f,0.f}, 0, 0, 0);
        acc2[1] = __builtin_amdgcn_mfma_f32_4x4x4bf16_1k(A2.v, B2[1].v, (f32x4){0.f,0.f,0.f,0.f}, 0, 0, 0);
      } else {
        acc2[0] = __builtin_amdgcn_mfma_f32_4x4x4bf16_1k(A2.v, B2[0].v, acc2[0], 0, 0, 0);
        acc2[1] = __builtin_amdgcn_mfma_f32_4x4x4bf16_1k(A2.v, B2[1].v, acc2[1], 0, 0, 0);
      }
      if (st + 5 < st1) {
        asm volatile("s_waitcnt lgkmcnt(0)" ::: "memory");
        qdma(st + 5, (st + 1 - st0) & 3);
      }
    };
    if (st0 < st1) {
#pragma unroll
      for (int k = 0; k < 4; k++) if (st0 + k < st1) qdma(st0 + k, k);
      if (st0 + 3 < st1) asm volatile("s_waitcnt vmcnt(3)" ::: "memory");
      else               asm volatile("s_waitcnt vmcnt(0)" ::: "memory");
      Ops X, Y;
      rdops(X, st0);
      if (st0 + 4 < st1) {
        asm volatile("s_waitcnt lgkmcnt(0)" ::: "memory");
        qdma(st0 + 4, 0);
      }
      int st = st0;
      for (; st + 1 < st1; st += 2) { body(st, X, Y); body(st + 1, Y, X); }
      if (st < st1) body(st, X, Y);
    }
    asm volatile("s_waitcnt vmcnt(0) lgkmcnt(0)" ::: "memory");
    {
      const uint4* wsrc = wF + ((w < 8) ? 4096 : 0) + w*256 + l;
#pragma unroll
      for (int k = 0; k < 4; k++) dma16(wsrc + k*64, ringb + (u32)k*1024u);
    }
    if (cur >= 0) flush(cur);
    lds_barrier();
    DSTAMP(3);
    for (int i = tid; i < 48*32; i += K2T) {
      int n = i >> 5, c4 = (i & 31) * 4;
      float4 s = make_float4(0.f,0.f,0.f,0.f);
      if (n < nn) {
        int a = sm.nstart[n] >> 2, bnd = sm.nstart[n+1] >> 2;
        if (bnd > a) {
          int wl = (16*a + 15) / nsteps, wh_ = (16*(bnd - 1) + 15) / nsteps;
          for (int ww = wl; ww <= wh_; ww++) {
            float4 v = *(const float4*)(sm.part + (n + ww)*132 + c4);
            s.x += v.x; s.y += v.y; s.z += v.z; s.w += v.w;
          }
        }
      }
      uint2 o; o.x = pk2(s.x, s.y); o.y = pk2(s.z, s.w);
      *(uint2*)(sM + n*136 + c4) = o;
    }
    if (tid < nn*3) {
      int n = tid / 3, k = tid - n*3;
      float s = 0.f;
      int a = sm.nstart[n] >> 2, bnd = sm.nstart[n+1] >> 2;
      if (bnd > a) {
        int wl = (16*a + 15) / nsteps, wh_ = (16*(bnd - 1) + 15) / nsteps;
        for (int ww = wl; ww <= wh_; ww++) s += sm.part[(n + ww)*132 + 128 + k];
      }
      out[(size_t)N*128 + (size_t)(n0+n)*3 + k] = f2bf(bf2f(bpos) + s);
    }
  } else {
    {
      const uint4* wsrc = wF + ((w < 8) ? 4096 : 0) + w*256 + l;
#pragma unroll
      for (int k = 0; k < 4; k++) dma16(wsrc + k*64, lds_addr(sm.sW + w*256) + (u32)k*1024u);
    }
    for (int n = w; n < 48; n += 16) {
      float a0 = 0.f, a1 = 0.f, px = 0.f, py = 0.f, pz = 0.f;
      if (n < nn) {
        const int node = n0 + n;
        const u32 pu0 = sm.sP[n*128 + 2*l], pu1 = sm.sP[n*128 + 2*l + 1];
        const float p0v = bf2f((u16)(pu0 & 0xFFFF)) + bf2f((u16)(pu0 >> 16)), p1v = bf2f((u16)(pu1 & 0xFFFF)) + bf2f((u16)(pu1 >> 16));
        float w50[5], w51[5];
        for (int k = 0; k < 5; k++) { w50[k] = bf2f(we[(256+k)*128 + 2*l]); w51[k] = bf2f(we[(256+k)*128 + 2*l+1]); }
        for (int e0 = 0; e0 < E; e0 += 64) {
          int e = e0 + l;
          int d = (e < E) ? dst[e] : -1;
          unsigned long long m = __ballot(d == node);
          while (m) {
            int bit = __ffsll((long long)m) - 1; m &= m - 1;
            int ee = e0 + bit; int s = src[ee];
            float dx = rbf(bf2f(pos[node*3+0]) - bf2f(pos[s*3+0]));
            float dy = rbf(bf2f(pos[node*3+1]) - bf2f(pos[s*3+1]));
            float dz = rbf(bf2f(pos[node*3+2]) - bf2f(pos[s*3+2]));
            float d2 = rbf(rbf(dx*dx) + rbf(dy*dy) + rbf(dz*dz));
            u32 qq = *(const u32*)(Qb + (size_t)s*128 + 2*l);
            float z0 = p0v + d2*w50[0], z1 = p1v + d2*w51[0];
            for (int k = 0; k < 4; k++) { float f = bf2f(ea[(size_t)ee*4+k]); z0 += f*w50[1+k]; z1 += f*w51[1+k]; }
            z0 += bf2f((u16)(qq & 0xFFFF)); z1 += bf2f((u16)(qq >> 16));
            float m0 = rbf(silu_f(z0)), m1 = rbf(silu_f(z1));
            a0 += m0; a1 += m1;
            float cs = m0*wxv[0] + m1*wxv[1];
            px += dx*cs; py += dy*cs; pz += dz*cs;
          }
        }
        for (int o = 1; o < 64; o <<= 1) { px += __shfl_xor(px, o); py += __shfl_xor(py, o); pz += __shfl_xor(pz, o); }
        if (l == 0) {
          out[(size_t)N*128 + (size_t)node*3 + 0] = f2bf(bf2f(pos[node*3+0]) + px);
          out[(size_t)N*128 + (size_t)node*3 + 1] = f2bf(bf2f(pos[node*3+1]) + py);
          out[(size_t)N*128 + (size_t)node*3 + 2] = f2bf(bf2f(pos[node*3+2]) + pz);
        }
      }
      *(u32*)(sM + n*136 + 2*l) = pk2(a0, a1);
    }
  }
  all_barrier();
  DSTAMP(4);
  float* sOut = sm.part;
  {
    f32x4 acc1[2];
    acc1[0] = (f32x4){0.f,0.f,0.f,0.f}; acc1[1] = (f32x4){0.f,0.f,0.f,0.f};
#pragma unroll
    for (int s = 0; s < 4; s++) {
      U4 Bh, Bm; Bh.u = sm.sW[2048 + (s*8 + tt)*64 + l]; Bm.u = sm.sW[(s*8 + tt)*64 + l];
#pragma unroll
      for (int i = 0; i < 2; i++) {
        if (i < nrt) {
          int rl = (rt0 + i)*16 + j;
          U4 Ah, Am;
          Ah.u = sm.sH[rl*16 + ((4*s + g) ^ j)];
          Am.u = *(const uint4*)(sM + rl*136 + 32*s + 8*g);
          acc1[i] = __builtin_amdgcn_mfma_f32_16x16x32_bf16(Ah.b, Bh.b, acc1[i], 0, 0, 0);
          acc1[i] = __builtin_amdgcn_mfma_f32_16x16x32_bf16(Am.b, Bm.b, acc1[i], 0, 0, 0);
        }
      }
    }
#pragma unroll
    for (int i = 0; i < 2; i++) if (i < nrt) {
#pragma unroll
      for (int r = 0; r < 4; r++) sOut[((rt0+i)*16 + 4*g + r)*132 + 8*j + tt] = acc1[i][r];
    }
  }
  lds_barrier();
  DSTAMP(5);
  for (int i = tid; i < nn*16; i += K2T) {
    int n = i >> 4, c8 = (i & 15) * 8;
    U4 hv2; hv2.u = sm.sH[n*16 + ((i & 15) ^ (n & 15))];
    U4 o;
#pragma unroll
    for (int k = 0; k < 8; k++) { float z = sOut[n*132 + c8 + k]; o.h[k] = f2bf(bf2f(hv2.h[k]) + silu_f(z)); }
    *(uint4*)(out + (size_t)(n0+n)*128 + c8) = o.u;
  }
  DSTAMP(6);
}

extern "C" void kernel_launch(void* const* d_in, const int* in_sizes, int n_in,
                              void* d_out, int out_size, void* d_ws, size_t ws_size,
                              hipStream_t stream) {
  const u16* h=(const u16*)d_in[0]; const u16* pos=(const u16*)d_in[1]; const u16* ea=(const u16*)d_in[2];
  const int* src=(const int*)d_in[3]; const int* dst=(const int*)d_in[4];
  const u16* we=(const u16*)d_in[5]; const u16* wx=(const u16*)d_in[6]; const u16* wh=(const u16*)d_in[7];
  int N = in_sizes[0]/128; int E = in_sizes[3];
  int nchunk = (E + CH - 1)/CH; int ngemm = (N + 127)/128; int np = (N + NB - 1)/NB;
  char* ws = (char*)d_ws; size_t o = 0;
  auto take = [&](size_t bytes){ void* p = ws + o; o += (bytes + 255) & ~(size_t)255; return p; };
  u16* Qb = (u16*)take((size_t)N*128*2);
  uint4* entA = (uint4*)take((size_t)nchunk*CH*16);
  u32* offT = (u32*)take((size_t)np*nchunk*4);
  uint4* wF = (uint4*)take(98304);
  k1_kernel<<<nchunk + ngemm + 12, K1T, 0, stream>>>(h,ea,src,dst,we,wh,Qb,entA,offT,wF,N,E,nchunk,ngemm,np
);
  k2_kernel<<<np, K2T, 0, stream>>>(h,pos,ea,src,dst,we,wx,Qb,entA,offT,wF,(u16*)d_out,N,E,nchunk,np
);
}
```

```cpp
#include <hip/hip_runtime.h>
#include <stdint.h>

typedef unsigned short u16;
typedef unsigned int u32;
typedef __bf16 bf16x8 __attribute__((ext_vector_type(8)));
typedef short s16x4 __attribute__((ext_vector_type(4)));
typedef float f32x4 __attribute__((ext_vector_type(4)));

constexpr int NB   = 40;
constexpr int CH   = 2048;
constexpr int K1T  = 512;
constexpr int K2T  = 1024;
constexpr int CAP  = 1024;
#define NSV 8
constexpr int NS   = NSV;
constexpr int NPMAX = 8192;

__device__ __forceinline__ float bf2f(u16 v){ return __uint_as_float(((u32)v)<<16); }
typedef __bf16 bf16x2 __attribute__((ext_vector_type(2)));
typedef float f32x2 __attribute__((ext_vector_type(2)));
__device__ __forceinline__ u32 pk2(float a, float b){
  f32x2 v = {a, b};
  bf16x2 r = __builtin_convertvector(v, bf16x2);
  return __builtin_bit_cast(u32, r);
}
__device__ __forceinline__ u16 f2bf(float f){ return (u16)(pk2(f, 0.f) & 0xFFFFu); }
__device__ __forceinline__ float rbf(float f){ return __uint_as_float(pk2(0.f, f) & 0xFFFF0000u); }
__device__ __forceinline__ float silu_f(float z){ return z * __builtin_amdgcn_rcpf(1.0f + __builtin_amdgcn_exp2f(-1.4426950408889634f * z)); }

template<int CTRL> __device__ __forceinline__ float dpp_add(float v){
  int x = __builtin_amdgcn_update_dpp(0, __float_as_int(v), CTRL, 0xF, 0xF, true);
  return v + __int_as_float(x);
}
__device__ __forceinline__ float wave_sum63(float v){
  v = dpp_add<0x111>(v);
  v = dpp_add<0x112>(v);
  v = dpp_add<0x114>(v);
  v = dpp_add<0x118>(v);
  v = dpp_add<0x142>(v);
  v = dpp_add<0x143>(v);
  return v;
}
struct __attribute__((packed, aligned(2))) Pk8 { unsigned long long v; };
__device__ __forceinline__ unsigned long long pos3(const u16* __restrict__ pos, int n, int N){
  const bool last = (n == N - 1);
  const Pk8* p = (const Pk8*)(pos + (size_t)n*3 - (last ? 1 : 0));
  unsigned long long v = p->v;
  return last ? (v >> 16) : v;
}

union U4 { uint4 u; bf16x8 b; u16 h[8]; };
union S4 { u32 u[2]; s16x4 v; u16 h[4]; };

#define STAMP(k, idx)
#define DSTAMP(idx)
__device__ __forceinline__ void lds_barrier(){ asm volatile("s_waitcnt lgkmcnt(0)\n\ts_barrier" ::: "memory"); }
__device__ __forceinline__ void all_barrier(){ asm volatile("s_waitcnt vmcnt(0) lgkmcnt(0)\n\ts_barrier" ::: "memory"); }

struct K1Part { u32 hist[NPMAX]; uint4 stA[CH]; u32 wtot[16]; };

__global__ __launch_bounds__(K1T) void k1_kernel(const u16* __restrict__ h,
    const u16* __restrict__ ea, const int* __restrict__ src, const int* __restrict__ dst,
    const u16* __restrict__ we, const u16* __restrict__ wh,
    u16* __restrict__ Qb, uint4* __restrict__ entA,
    u32* __restrict__ offT, uint4* __restrict__ wF, int N, int E, int nchunk, int ngemm, int np
)
{
  STAMP(0,0);
  __shared__ __attribute__((aligned(16))) char smem[sizeof(K1Part)];
  const int tid = threadIdx.x;
  const int b = blockIdx.x;
  if (b < nchunk) {
    K1Part& sp = *(K1Part*)smem;
    for (int i = tid; i < np; i += K1T) sp.hist[i] = 0;
    const int base = b * CH;
    int dv[4], sv[4]; uint2 av[4];
#pragma unroll
    for (int k = 0; k < 4; k++) {
      int e = min(base + k*K1T + tid, E-1);
      dv[k] = dst[e]; sv[k] = src[e]; av[k] = *(const uint2*)(ea + (size_t)e*4);
    }
    __syncthreads();
    STAMP(0,1);
    u32 slot[4]; int bn[4];
#pragma unroll
    for (int k = 0; k < 4; k++) {
      int e = base + k*K1T + tid;
      bn[k] = dv[k] / NB; slot[k] = 0;
      if (e < E) slot[k] = atomicAdd(&sp.hist[bn[k]], 1u);
    }
    lds_barrier();
    const int per = (np + K1T - 1) / K1T;
    const int i0 = min(tid * per, np), i1 = min(i0 + per, np);
    u32 loc = 0;
    for (int i = i0; i < i1; i++) loc += sp.hist[i];
    u32 incl = loc;
    const int l = tid & 63, w = tid >> 6;
#pragma unroll
    for (int o = 1; o < 64; o <<= 1) { u32 v = __shfl_up(incl, o); if (l >= o) incl += v; }
    if (l == 63) sp.wtot[w] = incl;
    lds_barrier();
    u32 run = incl - loc;
    for (int ww = 0; ww < w; ww++) run += sp.wtot[ww];
    for (int i = i0; i < i1; i++) { u32 c = sp.hist[i]; sp.hist[i] = run; offT[(size_t)i * nchunk + b] = run | (c << 16); run += c; }
    lds_barrier();
    STAMP(0,2);
#pragma unroll
    for (int k = 0; k < 4; k++) {
      int e = base + k*K1T + tid;
      if (e < E) {
        int idx = sp.hist[bn[k]] + slot[k];
        sp.stA[idx] = make_uint4(av[k].x, av[k].y, (u32)sv[k] | ((u32)(dv[k] - bn[k]*NB) << 26), (u32)e);
      }
    }
    __syncthreads();
    const int ne = min(CH, E - base);
#pragma unroll
    for (int k = 0; k < 4; k++) { int i = tid + k*K1T; if (i < ne) entA[(size_t)base + i] = sp.stA[i]; }
    STAMP(0,3);
  } else if (b < nchunk + ngemm) {
    u32* Wt32 = (u32*)smem;
    const int w = tid >> 6, l = tid & 63, g = l >> 4, j = l & 15;
    const int rowbase = (b - nchunk) * 64 + (w & 3) * 16;
    const int t0 = (w >> 2) * 4;
    U4 A[4];
    {
      int row = min(rowbase + j, N-1);
#pragma unroll
      for (int s = 0; s < 4; s++) A[s].u = *(const uint4*)(h + (size_t)row*128 + 32*s + 8*g);
    }
    {
      U4 r0[2], r1[2];
#pragma unroll
      for (int i4 = 0; i4 < 2; i4++) {
        int it = tid + i4*K1T;
        int oct = it & 15, kp = it >> 4;
        int k = 128 + 2*kp;
        r0[i4].u = *(const uint4*)(we + k*128 + oct*8);
        r1[i4].u = *(const uint4*)(we + (k+1)*128 + oct*8);
      }
#pragma unroll
      for (int i4 = 0; i4 < 2; i4++) {
        int it = tid + i4*K1T;
        int oct = it & 15, kp = it >> 4;
#pragma unroll
        for (int i = 0; i < 8; i++)
          Wt32[(i*16 + oct)*68 + kp] = (u32)r0[i4].h[i] | ((u32)r1[i4].h[i] << 16);
      }
    }
    __syncthreads();
    STAMP(0,1);
    f32x4 acc[4];
#pragma unroll
    for (int t = 0; t < 4; t++) acc[t] = (f32x4){0.f,0.f,0.f,0.f};
#pragma unroll
    for (int s = 0; s < 4; s++) {
#pragma unroll
      for (int t = 0; t < 4; t++) {
        U4 B; B.u = *(const uint4*)(Wt32 + ((t0 + t)*16 + j)*68 + 16*s + 4*g);
        acc[t] = __builtin_amdgcn_mfma_f32_16x16x32_bf16(A[s].b, B.b, acc[t], 0, 0, 0);
      }
    }
    STAMP(0,2);
#pragma unroll
    for (int r = 0; r < 4; r++) {
      int row = rowbase + 4*g + r;
      if (row < N) {
        uint2 v;
        v.x = pk2(acc[0][r], acc[1][r]); v.y = pk2(acc[2][r], acc[3][r]);
        *(uint2*)(Qb + (size_t)row*128 + 8*j + t0) = v;
      }
    }
    STAMP(0,3);
  } else {
    int it = (b - nchunk - ngemm) * K1T + tid;
    int l = it & 63, f = (it >> 6) & 31, part = it >> 11, t = f & 7, s = f >> 3, g = l >> 4, j = l & 15;
    const u16* wsrc = (part == 0) ? (we + 16*t + j) : (wh + (part - 1)*128*128 + 8*j + t);
    U4 v;
#pragma unroll
    for (int jj = 0; jj < 8; jj++) v.h[jj] = wsrc[(32*s + 8*g + jj)*128];
    wF[it] = v.u;
    STAMP(0,3);
  }
}

typedef __attribute__((address_space(3))) const void* lds_cptr;
__device__ __forceinline__ u32 lds_addr(const void* p){ return __builtin_amdgcn_readfirstlane((u32)(uintptr_t)(lds_cptr)p); }
__device__ __forceinline__ void dma16(const void* g, u32 lds){
  asm volatile("s_mov_b32 m0, %1\n\ts_nop 0\n\tglobal_load_lds_dwordx4 %0, off" :: "v"(g), "s"(lds) : "memory");
}
__device__ __forceinline__ void dma4(const void* g, u32 lds){
  asm volatile("s_mov_b32 m0, %1\n\ts_nop 0\n\tglobal_load_lds_dword %0, off" :: "v"(g), "s"(lds) : "memory");
}

struct K2Smem {
  uint4 sA[CAP];
  uint4 sW[4096];
  u32   sP[48*128];
  float part[(NB+16)*132];
  uint4 sH[48*16];
  u16   sG[4][CAP];
  u32   sSrc[CAP+4];
  int   ncount[64];
  int   nstart[72];
  int   nrow[64];
  int   flag[4];
  u32   dummy[64];
  u16   posd[128];
  unsigned char sNode[CAP/4];
};

__global__ __launch_bounds__(K2T) void k2_kernel(const u16* __restrict__ h, const u16* __restrict__ pos,
    const u16* __restrict__ ea, const int* __restrict__ src, const int* __restrict__ dst,
    const u16* __restrict__ we, const u16* __restrict__ wx,
    const u16* __restrict__ Qb,
    const uint4* __restrict__ entA, const u32* __restrict__ offT,
    const uint4* __restrict__ wF,
    u16* __restrict__ out, int N, int E, int nchunk, int np
)
{
  DSTAMP(0);
  __shared__ __attribute__((aligned(16))) K2Smem sm;
  const int tid = threadIdx.x, w = tid >> 6, l = tid & 63, g = l >> 4, j = l & 15;
  const int blk = blockIdx.x;
  const int n0 = blk * NB;
  const int nn = min(NB, N - n0);
  u16* const sM = (u16*)sm.sA;
  u32* const tmpE = (u32*)sm.part;
  uint4* const uA = (uint4*)(sm.part + 1024);
  u32* const uS = (u32*)(sm.part + 1024 + 4096);
  const int tt = w & 7;
  const int rt0 = (w >> 3) ? 2 : 0, nrt = (w >> 3) ? 1 : 2;

  u32 bposv, oc[3];
  {
    const u16* pb = pos + (size_t)n0*3 + min(tid, nn*3 - 1);
    asm volatile("global_load_ushort %0, %1, off" : "=v"(bposv) : "v"(pb) : "memory");
#pragma unroll
    for (int k = 0; k < 3; k++) {
      int c = (tid + k*K2T) >> 5;
      const u32* po = offT + (size_t)blk * nchunk + min(c, nchunk - 1);
      asm volatile("global_load_dword %0, %1, off" : "=v"(oc[k]) : "v"(po) : "memory");
    }
  }
#pragma unroll
  for (int k = 0; k < 3; k++) {
    const int id = min(w + 16*k, 43);
    if (id < 12) {
      const int r = id*4 + g;
      const int row = min(n0 + r, N - 1);
      dma16(h + (size_t)row*128 + (j ^ (r & 15))*8, lds_addr(sm.sH + id*64));
    } else {
      dma16(wF + (id - 12)*64 + l, lds_addr(sm.sW + (id - 12)*64));
    }
  }
  for (int i = tid; i < CAP; i += K2T) {
    sm.sA[i] = make_uint4(0,0,0,0); sm.sSrc[i] = 0;
    sm.sG[0][i] = 0; sm.sG[1][i] = 0; sm.sG[2][i] = 0; sm.sG[3][i] = 0;
  }
  if (tid < 64) { sm.ncount[tid] = 0; sm.nrow[tid] = 0; }
  if (tid < 4) { sm.flag[tid] = 0; sm.sSrc[CAP + tid] = 0; }
  asm volatile("s_waitcnt vmcnt(3)" : "+v"(bposv), "+v"(oc[0]), "+v"(oc[1]), "+v"(oc[2]) :: "memory");
  const u16 bpos = (u16)bposv;
#pragma unroll
  for (int k = 0; k < 3; k++) if (((tid + k*K2T) >> 5) >= nchunk) oc[k] = 0u;
  DSTAMP(10);
  uint4 IA[3]; bool val[3];
#pragma unroll
  for (int k = 0; k < 3; k++) {
    int i = tid + k*K2T, c = i >> 5, kk = i & 31;
    int cnt = (int)(oc[k] >> 16);
    val[k] = kk < cnt;
    size_t idx = val[k] ? ((size_t)c * CH + (oc[k] & 0xFFFFu) + kk) : (size_t)0;
    IA[k] = entA[idx];
  }
  const bool ovf = (nchunk > 96) || (N > (1 << 24)) || (oc[0] >> 16) > 32u || (oc[1] >> 16) > 32u || (oc[2] >> 16) > 32u;
  if (tid < 128) sm.posd[tid] = bpos;
  all_barrier();
  unsigned long long ps_[3];
#pragma unroll
  for (int k = 0; k < 3; k++) ps_[k] = pos3(pos, (int)(IA[k].z & 0x3FFFFFFu), N);
  DSTAMP(1);
  int dl[3], slot[3];
  if (ovf) sm.flag[0] = 1;
#pragma unroll
  for (int k = 0; k < 3; k++) {
    dl[k] = val[k] ? (int)(IA[k].z >> 26) : 0; slot[k] = 0;
    if (val[k]) slot[k] = atomicAdd(&sm.ncount[dl[k]], 1);
  }
  S4 W0[2], W1[2]; float wxv[2];
#pragma unroll
  for (int hh = 0; hh < 2; hh++) {
    int c = 2*l + hh;
#pragma unroll
    for (int k = 0; k < 4; k++) W0[hh].h[k] = we[(256+k)*128 + c];
    W1[hh].u[0] = 0; W1[hh].u[1] = we[260*128 + c];
    wxv[hh] = bf2f(wx[c]);
  }
#pragma unroll
  for (int i = 0; i < 2; i++) {
    if (i < nrt) {
      f32x4 ap = (f32x4){0.f,0.f,0.f,0.f};
      const int r = (rt0 + i)*16 + j;
#pragma unroll
      for (int s = 0; s < 4; s++) {
        U4 A, Be;
        A.u = sm.sH[r*16 + ((4*s + g) ^ j)];
        Be.u = sm.sW[(s*8 + tt)*64 + l];
        ap = __builtin_amdgcn_mfma_f32_16x16x32_bf16(A.b, Be.b, ap, 0, 0, 0);
      }
#pragma unroll
      for (int rr = 0; rr < 4; rr++) {
        float v = ap[rr]; u32 hi = pk2(0.f, v); float lo = v - __uint_as_float(hi & 0xFFFF0000u);
        sm.sP[((rt0+i)*16 + 4*g + rr)*128 + 16*tt + j] = (hi >> 16) | (pk2(0.f, lo) & 0xFFFF0000u);
      }
    }
  }
  lds_barrier();
  DSTAMP(7);
  int nst;
  {
    int c = (l < NB) ? sm.ncount[l] : 0;
    int cp = (c + 3) & ~3, incl = cp;
#pragma unroll
    for (int o = 1; o < 64; o <<= 1) { int v = __shfl_up(incl, o); if (l >= o) incl += v; }
    nst = incl - cp;
    if (w == 0) { sm.nstart[l] = nst; if (l == 63) sm.nstart[64] = incl; }
  }
  const int Tp = __builtin_amdgcn_readlane(nst, 63);
  const bool fast = (sm.flag[0] == 0) && (Tp <= CAP);
  DSTAMP(8);
  const int nsteps = fast ? (Tp >> 2) : 0;
  if (fast) {
#pragma unroll
    for (int k = 0; k < 3; k++) {
      const int p = __shfl(nst, dl[k]) + slot[k];
      if (val[k]) {
        const u16* pd = sm.posd + dl[k]*3;
        float dx = rbf(bf2f(pd[0]) - bf2f((u16)ps_[k]));
        float dy = rbf(bf2f(pd[1]) - bf2f((u16)(ps_[k] >> 16)));
        float dz = rbf(bf2f(pd[2]) - bf2f((u16)(ps_[k] >> 32)));
        float d2 = rbf(dx*dx) + rbf(dy*dy) + rbf(dz*dz);
        u32 dxy = pk2(dx, dy), dzd = pk2(dz, d2);
        tmpE[p] = IA[k].w;
        *(uint4*)&uA[p] = make_uint4((dzd >> 16) | (IA[k].x << 16), (IA[k].x >> 16) | (IA[k].y << 16),
                                     (IA[k].y >> 16) | (dxy << 16), (dxy >> 16) | (dzd << 16));
        uS[p] = (IA[k].z & 0x3FFFFFFu) << 8;
      }
    }
    if (tid < NB) {
      const int a = nst, cnt = sm.ncount[tid], bnd = a + ((cnt + 3) & ~3);
      for (int i = a >> 2; i < (bnd >> 2); i++) sm.sNode[i] = (unsigned char)tid;
      for (int i = a + cnt; i < bnd; i++) tmpE[i] = 0xFFFFFFFFu;
      if (cnt > 0) { const int wl = (16*(a >> 2) + 15) / nsteps, wh_ = (16*((bnd >> 2) - 1) + 15) / nsteps; sm.nrow[tid] = (tid + wl) | ((wh_ - wl + 1) << 16); }
    }
    lds_barrier();
    DSTAMP(9);
    if (tid < Tp) {
      const u32 ev = tmpE[tid];
      if (ev != 0xFFFFFFFFu) {
        const int n = sm.sNode[tid >> 2];
        const int a = sm.nstart[n], bnd = sm.nstart[n+1];
        const uint4 pa = uA[tid]; const u32 ps = uS[tid];
        int rank = 0;
        for (int i = a; i < bnd; i += 4) {
          uint4 t = *(const uint4*)(tmpE + i);
          rank += (t.x < ev) + (t.y < ev) + (t.z < ev) + (t.w < ev);
        }
        const int p = a + rank;
        sm.sA[p] = make_uint4(pa.x, pa.y, 0x3F803F80u, pa.z & 0xFFFFu);
        sm.sSrc[p] = ps;
        sm.sG[0][p] = 0x3F80; sm.sG[1][p] = (u16)(pa.z >> 16); sm.sG[2][p] = (u16)(pa.w & 0xFFFFu); sm.sG[3][p] = (u16)(pa.w >> 16);
      }
    }
  }
  DSTAMP(11);
  all_barrier();
  DSTAMP(12);
  const int st0 = __builtin_amdgcn_readfirstlane((w * nsteps) >> 4);
  const int st1 = __builtin_amdgcn_readfirstlane(((w + 1) * nsteps) >> 4);
  U4 Bh[4], Bm[4];
#pragma unroll
  for (int s = 0; s < 4; s++) { Bh[s].u = wF[2048 + (s*8 + tt)*64 + l]; Bm[s].u = wF[4096 + (s*8 + tt)*64 + l]; }
  DSTAMP(2);
  if (fast) {
    int cur = -1;
    S4 I4;
    I4.u[0] = ((l & 3) == 0) ? 0x00003F80u : (((l & 3) == 1) ? 0x3F800000u : 0u);
    I4.u[1] = ((l & 3) == 2) ? 0x00003F80u : (((l & 3) == 3) ? 0x3F800000u : 0u);
    f32x4 acc2[2];
    acc2[0] = (f32x4){0.f,0.f,0.f,0.f}; acc2[1] = (f32x4){0.f,0.f,0.f,0.f};
    auto flush = [&](int n) {
      float* pt = sm.part + (n + w) * 132;
      pt[2*l] = acc2[0][0]; pt[2*l + 1] = acc2[1][0];
      float px = acc2[0][1]*wxv[0] + acc2[1][1]*wxv[1];
      float py = acc2[0][2]*wxv[0] + acc2[1][2]*wxv[1];
      float pz = acc2[0][3]*wxv[0] + acc2[1][3]*wxv[1];
      asm volatile(
        "s_nop 1\n\t"
        "v_add_f32_dpp %0, %0, %0 row_shr:1 row_mask:0xf bank_mask:0xf bound_ctrl:1\n\t"
        "v_add_f32_dpp %1, %1, %1 row_shr:1 row_mask:0xf bank_mask:0xf bound_ctrl:1\n\t"
        "v_add_f32_dpp %2, %2, %2 row_shr:1 row_mask:0xf bank_mask:0xf bound_ctrl:1\n\t"
        "v_add_f32_dpp %0, %0, %0 row_shr:2 row_mask:0xf bank_mask:0xf bound_ctrl:1\n\t"
        "v_add_f32_dpp %1, %1, %1 row_shr:2 row_mask:0xf bank_mask:0xf bound_ctrl:1\n\t"
        "v_add_f32_dpp %2, %2, %2 row_shr:2 row_mask:0xf bank_mask:0xf bound_ctrl:1\n\t"
        "v_add_f32_dpp %0, %0, %0 row_shr:4 row_mask:0xf bank_mask:0xf bound_ctrl:1\n\t"
        "v_add_f32_dpp %1, %1, %1 row_shr:4 row_mask:0xf bank_mask:0xf bound_ctrl:1\n\t"
        "v_add_f32_dpp %2, %2, %2 row_shr:4 row_mask:0xf bank_mask:0xf bound_ctrl:1\n\t"
        "v_add_f32_dpp %0, %0, %0 row_shr:8 row_mask:0xf bank_mask:0xf bound_ctrl:1\n\t"
        "v_add_f32_dpp %1, %1, %1 row_shr:8 row_mask:0xf bank_mask:0xf bound_ctrl:1\n\t"
        "v_add_f32_dpp %2, %2, %2 row_shr:8 row_mask:0xf bank_mask:0xf bound_ctrl:1\n\t"
        "v_add_f32_dpp %0, %0, %0 row_bcast:15 row_mask:0xa bank_mask:0xf\n\t"
        "v_add_f32_dpp %1, %1, %1 row_bcast:15 row_mask:0xa bank_mask:0xf\n\t"
        "v_add_f32_dpp %2, %2, %2 row_bcast:15 row_mask:0xa bank_mask:0xf\n\t"
        "v_add_f32_dpp %0, %0, %0 row_bcast:31 row_mask:0xc bank_mask:0xf\n\t"
        "v_add_f32_dpp %1, %1, %1 row_bcast:31 row_mask:0xc bank_mask:0xf\n\t"
        "v_add_f32_dpp %2, %2, %2 row_bcast:31 row_mask:0xc bank_mask:0xf\n\t"
        : "+v"(px), "+v"(py), "+v"(pz));
      if (l == 63) { pt[128] = px; pt[129] = py; pt[130] = pz; }
    };
    const int ndv = (int)sm.sNode[min(st0 + l, CAP/4 - 1)];
    const int ndp = (int)sm.sNode[min(max(st0 + l - 1, 0), CAP/4 - 1)];
    const unsigned long long chg = __ballot(l == 0 || ndv != ndp);
    const u32 ringb = lds_addr(sm.sW + w*256);
    const u32* ringp = (const u32*)(sm.sW + w*256);
    auto qdma = [&](int st, int slot) {
      const u32 so = sm.sSrc[st*4 + (l >> 4)];
      dma16((const char*)Qb + (so + (u32)(l & 15)*16u), ringb + (u32)slot*1024u);
    };
    struct Ops { u32 q0, q1, q2, q3; uint4 a4; uint2 g2; };
    auto rdops = [&](Ops& o, int st) {
      const u32* rp = ringp + ((st - st0) & 3)*256 + l;
      o.q0 = rp[0]; o.q1 = rp[64]; o.q2 = rp[128]; o.q3 = rp[192];
      o.a4 = sm.sA[st*4 + (l & 3)];
      o.g2 = *(const uint2*)(&sm.sG[l & 3][st*4]);
    };
    auto body = [&](int st, Ops& c, Ops& nx) {
      if (st + 1 < st1) {
        if (st + 4 < st1) asm volatile("s_waitcnt vmcnt(3)" ::: "memory");
        else              asm volatile("s_waitcnt vmcnt(0)" ::: "memory");
        rdops(nx, st + 1);
      }
      const bool nw = (chg >> (st - st0)) & 1ull;
      if (nw) {
        if (cur >= 0) flush(cur);
        const int n = __builtin_amdgcn_readlane(ndv, st - st0);
        cur = n;
        W1[0].u[0] = sm.sP[n*128 + 2*l]; W1[1].u[0] = sm.sP[n*128 + 2*l + 1];
      }
      S4 B2[2];
      S4 Aa, Ab, A2;
      Aa.u[0] = c.a4.x; Aa.u[1] = c.a4.y; Ab.u[0] = c.a4.z; Ab.u[1] = c.a4.w;
      A2.u[0] = c.g2.x; A2.u[1] = c.g2.y;
#pragma unroll
      for (int hh = 0; hh < 2; hh++) {
        S4 Bq;
        Bq.u[0] = __builtin_amdgcn_perm(c.q1, c.q0, hh ? 0x07060302u : 0x05040100u);
        Bq.u[1] = __builtin_amdgcn_perm(c.q3, c.q2, hh ? 0x07060302u : 0x05040100u);
        f32x4 z = __builtin_amdgcn_mfma_f32_4x4x4bf16_1k(Aa.v, W0[hh].v, (f32x4){0.f,0.f,0.f,0.f}, 0, 0, 0);
        z = __builtin_amdgcn_mfma_f32_4x4x4bf16_1k(Ab.v, W1[hh].v, z, 0, 0, 0);
        z = __builtin_amdgcn_mfma_f32_4x4x4bf16_1k(I4.v, Bq.v, z, 0, 0, 0);
        f32x2 za = {z[0], z[1]}, zb = {z[2], z[3]};
        f32x2 ta = za * -1.4426950408889634f, tb = zb * -1.4426950408889634f;
        f32x2 xa = {__builtin_amdgcn_exp2f(ta[0]), __builtin_amdgcn_exp2f(ta[1])};
        f32x2 xb = {__builtin_amdgcn_exp2f(tb[0]), __builtin_amdgcn_exp2f(tb[1])};
        xa = xa + 1.0f; xb = xb + 1.0f;
        f32x2 ra = {__builtin_amdgcn_rcpf(xa[0]), __builtin_amdgcn_rcpf(xa[1])};
        f32x2 rb = {__builtin_amdgcn_rcpf(xb[0]), __builtin_amdgcn_rcpf(xb[1])};
        za = za * ra; zb = zb * rb;
        B2[hh].u[0] = pk2(za[0], za[1]); B2[hh].u[1] = pk2(zb[0], zb[1]);
      }
      if (nw) {
        acc2[0] = __builtin_amdgcn_mfma_f32_4x4x4bf16_1k(A2.v, B2[0].v, (f32x4){0.f,0.f,0.f,0.f}, 0, 0, 0);
        acc2[1] = __builtin_amdgcn_mfma_f32_4x4x4bf16_1k(A2.v, B2[1].v, (f32x4){0.f,0.f,0.f,0.f}, 0, 0, 0);
      } else {
        acc2[0] = __builtin_amdgcn_mfma_f32_4x4x4bf16_1k(A2.v, B2[0].v, acc2[0], 0, 0, 0);
        acc2[1] = __builtin_amdgcn_mfma_f32_4x4x4bf16_1k(A2.v, B2[1].v, acc2[1], 0, 0, 0);
      }
      if (st + 5 < st1) {
        asm volatile("s_waitcnt lgkmcnt(0)" ::: "memory");
        qdma(st + 5, (st + 1 - st0) & 3);
      }
    };
    if (st0 < st1) {
#pragma unroll
      for (int k = 0; k < 4; k++) if (st0 + k < st1) qdma(st0 + k, k);
      if (st0 + 3 < st1) asm volatile("s_waitcnt vmcnt(3)" ::: "memory");
      else               asm volatile("s_waitcnt vmcnt(0)" ::: "memory");
      Ops X, Y;
      rdops(X, st0);
      if (st0 + 4 < st1) {
        asm volatile("s_waitcnt lgkmcnt(0)" ::: "memory");
        qdma(st0 + 4, 0);
      }
      int st = st0;
      for (; st + 1 < st1; st += 2) { body(st, X, Y); body(st + 1, Y, X); }
      if (st < st1) body(st, X, Y);
    }
    if (cur >= 0) flush(cur);
    lds_barrier();
    DSTAMP(3);
    for (int i = tid; i < 48*32; i += K2T) {
      int n = i >> 5, c4 = (i & 31) * 4;
      float4 s = make_float4(0.f,0.f,0.f,0.f);
      const int nr = sm.nrow[n];
      for (int rr = 0; rr < (nr >> 16); rr++) {
        float4 v = *(const float4*)(sm.part + ((nr & 0xFFFF) + rr)*132 + c4);
        s.x += v.x; s.y += v.y; s.z += v.z; s.w += v.w;
      }
      uint2 o; o.x = pk2(s.x, s.y); o.y = pk2(s.z, s.w);
      *(uint2*)(sM + n*136 + c4) = o;
    }
    if (tid < nn*3) {
      int n = tid / 3, k = tid - n*3;
      const int nr = sm.nrow[n];
      float s = 0.f;
      for (int rr = 0; rr < (nr >> 16); rr++) s += sm.part[((nr & 0xFFFF) + rr)*132 + 128 + k];
      out[(size_t)N*128 + (size_t)(n0+n)*3 + k] = f2bf(bf2f(bpos) + s);
    }
  } else {
    for (int n = w; n < 48; n += 16) {
      float a0 = 0.f, a1 = 0.f, px = 0.f, py = 0.f, pz = 0.f;
      if (n < nn) {
        const int node = n0 + n;
        const u32 pu0 = sm.sP[n*128 + 2*l], pu1 = sm.sP[n*128 + 2*l + 1];
        const float p0v = bf2f((u16)(pu0 & 0xFFFF)) + bf2f((u16)(pu0 >> 16)), p1v = bf2f((u16)(pu1 & 0xFFFF)) + bf2f((u16)(pu1 >> 16));
        float w50[5], w51[5];
        for (int k = 0; k < 5; k++) { w50[k] = bf2f(we[(256+k)*128 + 2*l]); w51[k] = bf2f(we[(256+k)*128 + 2*l+1]); }
        for (int e0 = 0; e0 < E; e0 += 64) {
          int e = e0 + l;
          int d = (e < E) ? dst[e] : -1;
          unsigned long long m = __ballot(d == node);
          while (m) {
            int bit = __ffsll((long long)m) - 1; m &= m - 1;
            int ee = e0 + bit; int s = src[ee];
            float dx = rbf(bf2f(pos[node*3+0]) - bf2f(pos[s*3+0]));
            float dy = rbf(bf2f(pos[node*3+1]) - bf2f(pos[s*3+1]));
            float dz = rbf(bf2f(pos[node*3+2]) - bf2f(pos[s*3+2]));
            float d2 = rbf(rbf(dx*dx) + rbf(dy*dy) + rbf(dz*dz));
            u32 qq = *(const u32*)(Qb + (size_t)s*128 + 2*l);
            float z0 = p0v + d2*w50[0], z1 = p1v + d2*w51[0];
            for (int k = 0; k < 4; k++) { float f = bf2f(ea[(size_t)ee*4+k]); z0 += f*w50[1+k]; z1 += f*w51[1+k]; }
            z0 += bf2f((u16)(qq & 0xFFFF)); z1 += bf2f((u16)(qq >> 16));
            float m0 = rbf(silu_f(z0)), m1 = rbf(silu_f(z1));
            a0 += m0; a1 += m1;
            float cs = m0*wxv[0] + m1*wxv[1];
            px += dx*cs; py += dy*cs; pz += dz*cs;
          }
        }
        for (int o = 1; o < 64; o <<= 1) { px += __shfl_xor(px, o); py += __shfl_xor(py, o); pz += __shfl_xor(pz, o); }
        if (l == 0) {
          out[(size_t)N*128 + (size_t)node*3 + 0] = f2bf(bf2f(pos[node*3+0]) + px);
          out[(size_t)N*128 + (size_t)node*3 + 1] = f2bf(bf2f(pos[node*3+1]) + py);
          out[(size_t)N*128 + (size_t)node*3 + 2] = f2bf(bf2f(pos[node*3+2]) + pz);
        }
      }
      *(u32*)(sM + n*136 + 2*l) = pk2(a0, a1);
    }
  }
  lds_barrier();
  DSTAMP(4);
  float* sOut = sm.part;
  {
    f32x4 acc1[2];
    acc1[0] = (f32x4){0.f,0.f,0.f,0.f}; acc1[1] = (f32x4){0.f,0.f,0.f,0.f};
#pragma unroll
    for (int s = 0; s < 4; s++) {
#pragma unroll
      for (int i = 0; i < 2; i++) {
        if (i < nrt) {
          int rl = (rt0 + i)*16 + j;
          U4 Ah, Am;
          Ah.u = sm.sH[rl*16 + ((4*s + g) ^ j)];
          Am.u = *(const uint4*)(sM + rl*136 + 32*s + 8*g);
          acc1[i] = __builtin_amdgcn_mfma_f32_16x16x32_bf16(Ah.b, Bh[s].b, acc1[i], 0, 0, 0);
          acc1[i] = __builtin_amdgcn_mfma_f32_16x16x32_bf16(Am.b, Bm[s].b, acc1[i], 0, 0, 0);
        }
      }
    }
#pragma unroll
    for (int i = 0; i < 2; i++) if (i < nrt) {
#pragma unroll
      for (int r = 0; r < 4; r++) sOut[((rt0+i)*16 + 4*g + r)*132 + 8*j + tt] = acc1[i][r];
    }
  }
  lds_barrier();
  DSTAMP(5);
  for (int i = tid; i < nn*16; i += K2T) {
    int n = i >> 4, c8 = (i & 15) * 8;
    U4 hv2; hv2.u = sm.sH[n*16 + ((i & 15) ^ (n & 15))];
    U4 o;
#pragma unroll
    for (int k = 0; k < 8; k++) { float z = sOut[n*132 + c8 + k]; o.h[k] = f2bf(bf2f(hv2.h[k]) + silu_f(z)); }
    *(uint4*)(out + (size_t)(n0+n)*128 + c8) = o.u;
  }
  DSTAMP(6);
}

extern "C" void kernel_launch(void* const* d_in, const int* in_sizes, int n_in,
                              void* d_out, int out_size, void* d_ws, size_t ws_size,
                              hipStream_t stream) {
  const u16* h=(const u16*)d_in[0]; const u16* pos=(const u16*)d_in[1]; const u16* ea=(const u16*)d_in[2];
  const int* src=(const int*)d_in[3]; const int* dst=(const int*)d_in[4];
  const u16* we=(const u16*)d_in[5]; const u16* wx=(const u16*)d_in[6]; const u16* wh=(const u16*)d_in[7];
  int N = in_sizes[0]/128; int E = in_sizes[3];
  int nchunk = (E + CH - 1)/CH; int ngemm = (N + 63)/64; int np = (N + NB - 1)/NB;
  char* ws = (char*)d_ws; size_t o = 0;
  auto take = [&](size_t bytes){ void* p = ws + o; o += (bytes + 255) & ~(size_t)255; return p; };
  u16* Qb = (u16*)take((size_t)N*128*2);
  uint4* entA = (uint4*)take((size_t)nchunk*CH*16);
  u32* offT = (u32*)take((size_t)np*nchunk*4);
  uint4* wF = (uint4*)take(98304);
  k1_kernel<<<nchunk + ngemm + 12, K1T, 0, stream>>>(h,ea,src,dst,we,wh,Qb,entA,offT,wF,N,E,nchunk,ngemm,np
);
  k2_kernel<<<np, K2T, 0, stream>>>(h,pos,ea,src,dst,we,wx,Qb,entA,offT,wF,(u16*)d_out,N,E,nchunk,np
);
}
```

```cpp
#include <hip/hip_runtime.h>
#include <stdint.h>

typedef unsigned short u16;
typedef unsigned int u32;
typedef __bf16 bf16x8 __attribute__((ext_vector_type(8)));
typedef short s16x4 __attribute__((ext_vector_type(4)));
typedef float f32x4 __attribute__((ext_vector_type(4)));

constexpr int NB   = 40;
constexpr int CH   = 2048;
constexpr int K1T  = 512;
constexpr int K2T  = 1024;
constexpr int CAP  = 1024;
#define NSV 8
constexpr int NS   = NSV;
constexpr int NPMAX = 8192;

__device__ __forceinline__ float bf2f(u16 v){ return __uint_as_float(((u32)v)<<16); }
typedef __bf16 bf16x2 __attribute__((ext_vector_type(2)));
typedef float f32x2 __attribute__((ext_vector_type(2)));
__device__ __forceinline__ u32 pk2(float a, float b){
  f32x2 v = {a, b};
  bf16x2 r = __builtin_convertvector(v, bf16x2);
  return __builtin_bit_cast(u32, r);
}
__device__ __forceinline__ u16 f2bf(float f){ return (u16)(pk2(f, 0.f) & 0xFFFFu); }
__device__ __forceinline__ float rbf(float f){ return __uint_as_float(pk2(0.f, f) & 0xFFFF0000u); }
__device__ __forceinline__ float silu_f(float z){ return z * __builtin_amdgcn_rcpf(1.0f + __builtin_amdgcn_exp2f(-1.4426950408889634f * z)); }

template<int CTRL> __device__ __forceinline__ float dpp_add(float v){
  int x = __builtin_amdgcn_update_dpp(0, __float_as_int(v), CTRL, 0xF, 0xF, true);
  return v + __int_as_float(x);
}
__device__ __forceinline__ float wave_sum63(float v){
  v = dpp_add<0x111>(v);
  v = dpp_add<0x112>(v);
  v = dpp_add<0x114>(v);
  v = dpp_add<0x118>(v);
  v = dpp_add<0x142>(v);
  v = dpp_add<0x143>(v);
  return v;
}
__device__ __forceinline__ int wave_scan_incl(int v){
  v += __builtin_amdgcn_update_dpp(0, v, 0x111, 0xF, 0xF, true);
  v += __builtin_amdgcn_update_dpp(0, v, 0x112, 0xF, 0xF, true);
  v += __builtin_amdgcn_update_dpp(0, v, 0x114, 0xF, 0xF, true);
  v += __builtin_amdgcn_update_dpp(0, v, 0x118, 0xF, 0xF, true);
  v += __builtin_amdgcn_update_dpp(0, v, 0x142, 0xA, 0xF, false);
  v += __builtin_amdgcn_update_dpp(0, v, 0x143, 0xC, 0xF, false);
  return v;
}
struct __attribute__((packed, aligned(2))) Pk8 { unsigned long long v; };
__device__ __forceinline__ unsigned long long pos3(const u16* __restrict__ pos, int n, int N){
  const bool last = (n == N - 1);
  const Pk8* p = (const Pk8*)(pos + (size_t)n*3 - (last ? 1 : 0));
  unsigned long long v = p->v;
  return last ? (v >> 16) : v;
}

union U4 { uint4 u; bf16x8 b; u16 h[8]; };
union S4 { u32 u[2]; s16x4 v; u16 h[4]; };

#define STAMP(k, idx)
#define DSTAMP(idx)
__device__ __forceinline__ void lds_barrier(){ asm volatile("s_waitcnt lgkmcnt(0)\n\ts_barrier" ::: "memory"); }
__device__ __forceinline__ void all_barrier(){ asm volatile("s_waitcnt vmcnt(0) lgkmcnt(0)\n\ts_barrier" ::: "memory"); }

struct K1Part { u32 hist[NPMAX]; uint4 stA[CH]; u32 wtot[16]; };

__global__ __launch_bounds__(K1T) void k1_kernel(const u16* __restrict__ h,
    const u16* __restrict__ ea, const int* __restrict__ src, const int* __restrict__ dst,
    const u16* __restrict__ we, const u16* __restrict__ wh,
    u16* __restrict__ Qb, uint4* __restrict__ entA,
    u32* __restrict__ offT, uint4* __restrict__ wF, int N, int E, int nchunk, int ngemm, int np
)
{
  STAMP(0,0);
  __shared__ __attribute__((aligned(16))) char smem[sizeof(K1Part)];
  const int tid = threadIdx.x;
  const int b = blockIdx.x;
  if (b < nchunk) {
    K1Part& sp = *(K1Part*)smem;
    for (int i = tid; i < np; i += K1T) sp.hist[i] = 0;
    const int base = b * CH;
    int dv[4], sv[4]; uint2 av[4];
#pragma unroll
    for (int k = 0; k < 4; k++) {
      int e = min(base + k*K1T + tid, E-1);
      dv[k] = dst[e];
    }
#pragma unroll
    for (int k = 0; k < 4; k++) {
      int e = min(base + k*K1T + tid, E-1);
      sv[k] = src[e]; av[k] = *(const uint2*)(ea + (size_t)e*4);
    }
    lds_barrier();
    STAMP(0,1);
    u32 slot[4]; int bn[4];
#pragma unroll
    for (int k = 0; k < 4; k++) {
      int e = base + k*K1T + tid;
      bn[k] = dv[k] / NB; slot[k] = 0;
      if (e < E) slot[k] = atomicAdd(&sp.hist[bn[k]], 1u);
    }
    lds_barrier();
    const int per = (np + K1T - 1) / K1T;
    const int i0 = min(tid * per, np), i1 = min(i0 + per, np);
    u32 loc = 0;
    for (int i = i0; i < i1; i++) loc += sp.hist[i];
    const int l = tid & 63, w = tid >> 6;
    const u32 incl = (u32)wave_scan_incl((int)loc);
    if (l == 63) sp.wtot[w] = incl;
    lds_barrier();
    u32 run = incl - loc;
    {
      const uint4 ta = *(const uint4*)sp.wtot, tb = *(const uint4*)(sp.wtot + 4);
      run += (w > 0 ? ta.x : 0u) + (w > 1 ? ta.y : 0u) + (w > 2 ? ta.z : 0u) + (w > 3 ? ta.w : 0u)
           + (w > 4 ? tb.x : 0u) + (w > 5 ? tb.y : 0u) + (w > 6 ? tb.z : 0u);
    }
    for (int i = i0; i < i1; i++) { u32 c = sp.hist[i]; sp.hist[i] = run; offT[(size_t)i * nchunk + b] = run | (c << 16); run += c; }
    lds_barrier();
    STAMP(0,2);
#pragma unroll
    for (int k = 0; k < 4; k++) {
      int e = base + k*K1T + tid;
      if (e < E) {
        int idx = sp.hist[bn[k]] + slot[k];
        sp.stA[idx] = make_uint4(av[k].x, av[k].y, (u32)sv[k] | ((u32)(dv[k] - bn[k]*NB) << 26), (u32)e);
      }
    }
    __syncthreads();
    const int ne = min(CH, E - base);
#pragma unroll
    for (int k = 0; k < 4; k++) { int i = tid + k*K1T; if (i < ne) entA[(size_t)base + i] = sp.stA[i]; }
    STAMP(0,3);
  } else if (b < nchunk + ngemm) {
    u32* Wt32 = (u32*)smem;
    const int w = tid >> 6, l = tid & 63, g = l >> 4, j = l & 15;
    const int rowbase = (b - nchunk) * 64 + (w & 3) * 16;
    const int t0 = (w >> 2) * 4;
    U4 A[4];
    {
      int row = min(rowbase + j, N-1);
#pragma unroll
      for (int s = 0; s < 4; s++) A[s].u = *(const uint4*)(h + (size_t)row*128 + 32*s + 8*g);
    }
    {
      U4 r0[2], r1[2];
#pragma unroll
      for (int i4 = 0; i4 < 2; i4++) {
        int it = tid + i4*K1T;
        int oct = it & 15, kp = it >> 4;
        int k = 128 + 2*kp;
        r0[i4].u = *(const uint4*)(we + k*128 + oct*8);
        r1[i4].u = *(const uint4*)(we + (k+1)*128 + oct*8);
      }
#pragma unroll
      for (int i4 = 0; i4 < 2; i4++) {
        int it = tid + i4*K1T;
        int oct = it & 15, kp = it >> 4;
#pragma unroll
        for (int i = 0; i < 8; i++)
          Wt32[(i*16 + oct)*68 + kp] = (u32)r0[i4].h[i] | ((u32)r1[i4].h[i] << 16);
      }
    }
    __syncthreads();
    STAMP(0,1);
    f32x4 acc[4];
#pragma unroll
    for (int t = 0; t < 4; t++) acc[t] = (f32x4){0.f,0.f,0.f,0.f};
#pragma unroll
    for (int s = 0; s < 4; s++) {
#pragma unroll
      for (int t = 0; t < 4; t++) {
        U4 B; B.u = *(const uint4*)(Wt32 + ((t0 + t)*16 + j)*68 + 16*s + 4*g);
        acc[t] = __builtin_amdgcn_mfma_f32_16x16x32_bf16(A[s].b, B.b, acc[t], 0, 0, 0);
      }
    }
    STAMP(0,2);
#pragma unroll
    for (int r = 0; r < 4; r++) {
      int row = rowbase + 4*g + r;
      if (row < N) {
        uint2 v;
        v.x = pk2(acc[0][r], acc[1][r]); v.y = pk2(acc[2][r], acc[3][r]);
        *(uint2*)(Qb + (size_t)row*128 + 8*j + t0) = v;
      }
    }
    STAMP(0,3);
  } else {
    int it = (b - nchunk - ngemm) * K1T + tid;
    int l = it & 63, f = (it >> 6) & 31, part = it >> 11, t = f & 7, s = f >> 3, g = l >> 4, j = l & 15;
    const u16* wsrc = (part == 0) ? (we + 16*t + j) : (wh + (part - 1)*128*128 + 8*j + t);
    U4 v;
#pragma unroll
    for (int jj = 0; jj < 8; jj++) v.h[jj] = wsrc[(32*s + 8*g + jj)*128];
    wF[it] = v.u;
    STAMP(0,3);
  }
}

typedef __attribute__((address_space(3))) const void* lds_cptr;
__device__ __forceinline__ u32 lds_addr(const void* p){ return __builtin_amdgcn_readfirstlane((u32)(uintptr_t)(lds_cptr)p); }
__device__ __forceinline__ void dma16(const void* g, u32 lds){
  asm volatile("s_mov_b32 m0, %1\n\ts_nop 0\n\tglobal_load_lds_dwordx4 %0, off" :: "v"(g), "s"(lds) : "memory");
}
__device__ __forceinline__ void dma4(const void* g, u32 lds){
  asm volatile("s_mov_b32 m0, %1\n\ts_nop 0\n\tglobal_load_lds_dword %0, off" :: "v"(g), "s"(lds) : "memory");
}

struct K2Smem {
  uint4 sA[CAP];
  uint4 sW[4096];
  u32   sP[48*128];
  float part[(NB+16)*132];
  uint4 sH[48*16];
  u16   sG[4][CAP];
  u32   sSrc[CAP+4];
  int   ncount[64];
  int   nstart[72];
  int   nrow[64];
  int   flag[4];
  u32   dummy[64];
  u16   posd[128];
  unsigned char sNode[CAP/4];
};

__global__ __launch_bounds__(K2T) void k2_kernel(const u16* __restrict__ h, const u16* __restrict__ pos,
    const u16* __restrict__ ea, const int* __restrict__ src, const int* __restrict__ dst,
    const u16* __restrict__ we, const u16* __restrict__ wx,
    const u16* __restrict__ Qb,
    const uint4* __restrict__ entA, const u32* __restrict__ offT,
    const uint4* __restrict__ wF,
    u16* __restrict__ out, int N, int E, int nchunk, int np
)
{
  DSTAMP(0);
  __shared__ __attribute__((aligned(16))) K2Smem sm;
  const int tid = threadIdx.x, w = tid >> 6, l = tid & 63, g = l >> 4, j = l & 15;
  const int blk = blockIdx.x;
  const int n0 = blk * NB;
  const int nn = min(NB, N - n0);
  u16* const sM = (u16*)sm.sA;
  u32* const tmpE = (u32*)sm.part;
  uint4* const uA = (uint4*)(sm.part + 1024);
  u32* const uS = (u32*)(sm.part + 1024 + 4096);
  const int tt = w & 7;
  const int rt0 = (w >> 3) ? 2 : 0, nrt = (w >> 3) ? 1 : 2;

  u32 bposv, oc[3];
  {
    const u16* pb = pos + (size_t)n0*3 + min(tid, nn*3 - 1);
    asm volatile("global_load_ushort %0, %1, off" : "=v"(bposv) : "v"(pb) : "memory");
#pragma unroll
    for (int k = 0; k < 3; k++) {
      int c = (tid + k*K2T) >> 5;
      const u32* po = offT + (size_t)blk * nchunk + min(c, nchunk - 1);
      asm volatile("global_load_dword %0, %1, off" : "=v"(oc[k]) : "v"(po) : "memory");
    }
  }
#pragma unroll
  for (int k = 0; k < 3; k++) {
    const int id = min(w + 16*k, 43);
    if (id < 12) {
      const int r = id*4 + g;
      const int row = min(n0 + r, N - 1);
      dma16(h + (size_t)row*128 + (j ^ (r & 15))*8, lds_addr(sm.sH + id*64));
    } else {
      dma16(wF + (id - 12)*64 + l, lds_addr(sm.sW + (id - 12)*64));
    }
  }
  for (int i = tid; i < CAP; i += K2T) {
    sm.sA[i] = make_uint4(0,0,0,0); sm.sSrc[i] = 0;
    sm.sG[0][i] = 0; sm.sG[1][i] = 0; sm.sG[2][i] = 0; sm.sG[3][i] = 0;
  }
  if (tid < 64) { sm.ncount[tid] = 0; sm.nrow[tid] = 0; }
  if (tid < 4) { sm.flag[tid] = 0; sm.sSrc[CAP + tid] = 0; }
  asm volatile("s_waitcnt vmcnt(3)" : "+v"(bposv), "+v"(oc[0]), "+v"(oc[1]), "+v"(oc[2]) :: "memory");
  const u16 bpos = (u16)bposv;
#pragma unroll
  for (int k = 0; k < 3; k++) if (((tid + k*K2T) >> 5) >= nchunk) oc[k] = 0u;
  DSTAMP(10);
  uint4 IA[3]; bool val[3];
#pragma unroll
  for (int k = 0; k < 3; k++) {
    int i = tid + k*K2T, c = i >> 5, kk = i & 31;
    int cnt = (int)(oc[k] >> 16);
    val[k] = kk < cnt;
    size_t idx = val[k] ? ((size_t)c * CH + (oc[k] & 0xFFFFu) + kk) : (size_t)0;
    IA[k] = entA[idx];
  }
  const bool ovf = (nchunk > 96) || (N > (1 << 24)) || (oc[0] >> 16) > 32u || (oc[1] >> 16) > 32u || (oc[2] >> 16) > 32u;
  if (tid < 128) sm.posd[tid] = bpos;
  all_barrier();
  unsigned long long ps_[3];
#pragma unroll
  for (int k = 0; k < 3; k++) ps_[k] = pos3(pos, (int)(IA[k].z & 0x3FFFFFFu), N);
  DSTAMP(1);
  int dl[3], slot[3];
  if (ovf) sm.flag[0] = 1;
#pragma unroll
  for (int k = 0; k < 3; k++) {
    dl[k] = val[k] ? (int)(IA[k].z >> 26) : 0; slot[k] = 0;
    if (val[k]) slot[k] = atomicAdd(&sm.ncount[dl[k]], 1);
  }
  S4 W0[2], W1[2]; float wxv[2];
#pragma unroll
  for (int hh = 0; hh < 2; hh++) {
    int c = 2*l + hh;
#pragma unroll
    for (int k = 0; k < 4; k++) W0[hh].h[k] = we[(256+k)*128 + c];
    W1[hh].u[0] = 0; W1[hh].u[1] = we[260*128 + c];
    wxv[hh] = bf2f(wx[c]);
  }
#pragma unroll
  for (int i = 0; i < 2; i++) {
    if (i < nrt) {
      f32x4 ap = (f32x4){0.f,0.f,0.f,0.f};
      const int r = (rt0 + i)*16 + j;
#pragma unroll
      for (int s = 0; s < 4; s++) {
        U4 A, Be;
        A.u = sm.sH[r*16 + ((4*s + g) ^ j)];
        Be.u = sm.sW[(s*8 + tt)*64 + l];
        ap = __builtin_amdgcn_mfma_f32_16x16x32_bf16(A.b, Be.b, ap, 0, 0, 0);
      }
#pragma unroll
      for (int rr = 0; rr < 4; rr++) {
        float v = ap[rr]; u32 hi = pk2(0.f, v); float lo = v - __uint_as_float(hi & 0xFFFF0000u);
        sm.sP[((rt0+i)*16 + 4*g + rr)*128 + 16*tt + j] = (hi >> 16) | (pk2(0.f, lo) & 0xFFFF0000u);
      }
    }
  }
  lds_barrier();
  DSTAMP(7);
  int nst;
  {
    int c = (l < NB) ? sm.ncount[l] : 0;
    int cp = (c + 3) & ~3;
    const int incl = wave_scan_incl(cp);
    nst = incl - cp;
    if (w == 0) { sm.nstart[l] = nst; if (l == 63) sm.nstart[64] = incl; }
  }
  const int Tp = __builtin_amdgcn_readlane(nst, 63);
  const bool fast = (sm.flag[0] == 0) && (Tp <= CAP);
  DSTAMP(8);
  const int nsteps = fast ? (Tp >> 2) : 0;
  if (fast) {
#pragma unroll
    for (int k = 0; k < 3; k++) {
      const int p = __shfl(nst, dl[k]) + slot[k];
      if (val[k]) {
        const u16* pd = sm.posd + dl[k]*3;
        float dx = rbf(bf2f(pd[0]) - bf2f((u16)ps_[k]));
        float dy = rbf(bf2f(pd[1]) - bf2f((u16)(ps_[k] >> 16)));
        float dz = rbf(bf2f(pd[2]) - bf2f((u16)(ps_[k] >> 32)));
        float d2 = rbf(dx*dx) + rbf(dy*dy) + rbf(dz*dz);
        u32 dxy = pk2(dx, dy), dzd = pk2(dz, d2);
        tmpE[p] = IA[k].w;
        *(uint4*)&uA[p] = make_uint4((dzd >> 16) | (IA[k].x << 16), (IA[k].x >> 16) | (IA[k].y << 16),
                                     (IA[k].y >> 16) | (dxy << 16), (dxy >> 16) | (dzd << 16));
        uS[p] = (IA[k].z & 0x3FFFFFFu) << 8;
      }
    }
    if (w == 15 && l < NB) {
      const int a = nst, cnt = sm.ncount[l], bnd = a + ((cnt + 3) & ~3);
      for (int i = a >> 2; i < (bnd >> 2); i++) sm.sNode[i] = (unsigned char)l;
      for (int i = a + cnt; i < bnd; i++) tmpE[i] = 0xFFFFFFFFu;
      if (cnt > 0) { const int wl = (16*(a >> 2) + 15) / nsteps, wh_ = (16*((bnd >> 2) - 1) + 15) / nsteps; sm.nrow[l] = (l + wl) | ((wh_ - wl + 1) << 16); }
    }
    lds_barrier();
    DSTAMP(9);
    if (tid < Tp) {
      const u32 ev = tmpE[tid];
      if (ev != 0xFFFFFFFFu) {
        const int n = sm.sNode[tid >> 2];
        const int a = sm.nstart[n], bnd = sm.nstart[n+1];
        const uint4 pa = uA[tid]; const u32 ps = uS[tid];
        int rank = 0;
        for (int i = a; i < bnd; i += 4) {
          uint4 t = *(const uint4*)(tmpE + i);
          rank += (t.x < ev) + (t.y < ev) + (t.z < ev) + (t.w < ev);
        }
        const int p = a + rank;
        sm.sA[p] = make_uint4(pa.x, pa.y, 0x3F803F80u, pa.z & 0xFFFFu);
        sm.sSrc[p] = ps;
        sm.sG[0][p] = 0x3F80; sm.sG[1][p] = (u16)(pa.z >> 16); sm.sG[2][p] = (u16)(pa.w & 0xFFFFu); sm.sG[3][p] = (u16)(pa.w >> 16);
      }
    }
  }
  DSTAMP(11);
  all_barrier();
  DSTAMP(12);
  const int st0 = __builtin_amdgcn_readfirstlane((w * nsteps) >> 4);
  const int st1 = __builtin_amdgcn_readfirstlane(((w + 1) * nsteps) >> 4);
  U4 Bh[4], Bm[4];
#pragma unroll
  for (int s = 0; s < 4; s++) { Bh[s].u = wF[2048 + (s*8 + tt)*64 + l]; Bm[s].u = wF[4096 + (s*8 + tt)*64 + l]; }
  DSTAMP(2);
  if (fast) {
    int cur = -1;
    S4 I4;
    I4.u[0] = ((l & 3) == 0) ? 0x00003F80u : (((l & 3) == 1) ? 0x3F800000u : 0u);
    I4.u[1] = ((l & 3) == 2) ? 0x00003F80u : (((l & 3) == 3) ? 0x3F800000u : 0u);
    f32x4 acc2[2];
    acc2[0] = (f32x4){0.f,0.f,0.f,0.f}; acc2[1] = (f32x4){0.f,0.f,0.f,0.f};
    auto flush = [&](int n) {
      float* pt = sm.part + (n + w) * 132;
      pt[2*l] = acc2[0][0]; pt[2*l + 1] = acc2[1][0];
      float px = acc2[0][1]*wxv[0] + acc2[1][1]*wxv[1];
      float py = acc2[0][2]*wxv[0] + acc2[1][2]*wxv[1];
      float pz = acc2[0][3]*wxv[0] + acc2[1][3]*wxv[1];
      asm volatile(
        "s_nop 1\n\t"
        "v_add_f32_dpp %0, %0, %0 row_shr:1 row_mask:0xf bank_mask:0xf bound_ctrl:1\n\t"
        "v_add_f32_dpp %1, %1, %1 row_shr:1 row_mask:0xf bank_mask:0xf bound_ctrl:1\n\t"
        "v_add_f32_dpp %2, %2, %2 row_shr:1 row_mask:0xf bank_mask:0xf bound_ctrl:1\n\t"
        "v_add_f32_dpp %0, %0, %0 row_shr:2 row_mask:0xf bank_mask:0xf bound_ctrl:1\n\t"
        "v_add_f32_dpp %1, %1, %1 row_shr:2 row_mask:0xf bank_mask:0xf bound_ctrl:1\n\t"
        "v_add_f32_dpp %2, %2, %2 row_shr:2 row_mask:0xf bank_mask:0xf bound_ctrl:1\n\t"
        "v_add_f32_dpp %0, %0, %0 row_shr:4 row_mask:0xf bank_mask:0xf bound_ctrl:1\n\t"
        "v_add_f32_dpp %1, %1, %1 row_shr:4 row_mask:0xf bank_mask:0xf bound_ctrl:1\n\t"
        "v_add_f32_dpp %2, %2, %2 row_shr:4 row_mask:0xf bank_mask:0xf bound_ctrl:1\n\t"
        "v_add_f32_dpp %0, %0, %0 row_shr:8 row_mask:0xf bank_mask:0xf bound_ctrl:1\n\t"
        "v_add_f32_dpp %1, %1, %1 row_shr:8 row_mask:0xf bank_mask:0xf bound_ctrl:1\n\t"
        "v_add_f32_dpp %2, %2, %2 row_shr:8 row_mask:0xf bank_mask:0xf bound_ctrl:1\n\t"
        "v_add_f32_dpp %0, %0, %0 row_bcast:15 row_mask:0xa bank_mask:0xf\n\t"
        "v_add_f32_dpp %1, %1, %1 row_bcast:15 row_mask:0xa bank_mask:0xf\n\t"
        "v_add_f32_dpp %2, %2, %2 row_bcast:15 row_mask:0xa bank_mask:0xf\n\t"
        "v_add_f32_dpp %0, %0, %0 row_bcast:31 row_mask:0xc bank_mask:0xf\n\t"
        "v_add_f32_dpp %1, %1, %1 row_bcast:31 row_mask:0xc bank_mask:0xf\n\t"
        "v_add_f32_dpp %2, %2, %2 row_bcast:31 row_mask:0xc bank_mask:0xf\n\t"
        : "+v"(px), "+v"(py), "+v"(pz));
      if (l == 63) { pt[128] = px; pt[129] = py; pt[130] = pz; }
    };
    const int ndv = (int)sm.sNode[min(st0 + l, CAP/4 - 1)];
    const int ndp = (int)sm.sNode[min(max(st0 + l - 1, 0), CAP/4 - 1)];
    const unsigned long long chg = __ballot(l == 0 || ndv != ndp);
    const u32 ringb = lds_addr(sm.sW + w*256);
    const u32* ringp = (const u32*)(sm.sW + w*256);
    auto qdma = [&](int st, int slot) {
      const u32 so = sm.sSrc[st*4 + (l >> 4)];
      dma16((const char*)Qb + (so + (u32)(l & 15)*16u), ringb + (u32)slot*1024u);
    };
    struct Ops { u32 q0, q1, q2, q3; uint4 a4; uint2 g2; };
    auto rdops = [&](Ops& o, int st) {
      const u32* rp = ringp + ((st - st0) & 3)*256 + l;
      o.q0 = rp[0]; o.q1 = rp[64]; o.q2 = rp[128]; o.q3 = rp[192];
      o.a4 = sm.sA[st*4 + (l & 3)];
      o.g2 = *(const uint2*)(&sm.sG[l & 3][st*4]);
    };
    auto body = [&](int st, Ops& c, Ops& nx) {
      if (st + 1 < st1) {
        if (st + 4 < st1) asm volatile("s_waitcnt vmcnt(3)" ::: "memory");
        else              asm volatile("s_waitcnt vmcnt(0)" ::: "memory");
        rdops(nx, st + 1);
      }
      const bool nw = (chg >> (st - st0)) & 1ull;
      if (nw) {
        if (cur >= 0) flush(cur);
        const int n = __builtin_amdgcn_readlane(ndv, st - st0);
        cur = n;
        W1[0].u[0] = sm.sP[n*128 + 2*l]; W1[1].u[0] = sm.sP[n*128 + 2*l + 1];
      }
      S4 B2[2];
      S4 Aa, Ab, A2;
      Aa.u[0] = c.a4.x; Aa.u[1] = c.a4.y; Ab.u[0] = c.a4.z; Ab.u[1] = c.a4.w;
      A2.u[0] = c.g2.x; A2.u[1] = c.g2.y;
#pragma unroll
      for (int hh = 0; hh < 2; hh++) {
        S4 Bq;
        Bq.u[0] = __builtin_amdgcn_perm(c.q1, c.q0, hh ? 0x07060302u : 0x05040100u);
        Bq.u[1] = __builtin_amdgcn_perm(c.q3, c.q2, hh ? 0x07060302u : 0x05040100u);
        f32x4 z = __builtin_amdgcn_mfma_f32_4x4x4bf16_1k(Aa.v, W0[hh].v, (f32x4){0.f,0.f,0.f,0.f}, 0, 0, 0);
        z = __builtin_amdgcn_mfma_f32_4x4x4bf16_1k(Ab.v, W1[hh].v, z, 0, 0, 0);
        z = __builtin_amdgcn_mfma_f32_4x4x4bf16_1k(I4.v, Bq.v, z, 0, 0, 0);
        f32x2 za = {z[0], z[1]}, zb = {z[2], z[3]};
        f32x2 ta = za * -1.4426950408889634f, tb = zb * -1.4426950408889634f;
        f32x2 xa = {__builtin_amdgcn_exp2f(ta[0]), __builtin_amdgcn_exp2f(ta[1])};
        f32x2 xb = {__builtin_amdgcn_exp2f(tb[0]), __builtin_amdgcn_exp2f(tb[1])};
        xa = xa + 1.0f; xb = xb + 1.0f;
        f32x2 ra = {__builtin_amdgcn_rcpf(xa[0]), __builtin_amdgcn_rcpf(xa[1])};
        f32x2 rb = {__builtin_amdgcn_rcpf(xb[0]), __builtin_amdgcn_rcpf(xb[1])};
        za = za * ra; zb = zb * rb;
        B2[hh].u[0] = pk2(za[0], za[1]); B2[hh].u[1] = pk2(zb[0], zb[1]);
      }
      if (nw) {
        acc2[0] = __builtin_amdgcn_mfma_f32_4x4x4bf16_1k(A2.v, B2[0].v, (f32x4){0.f,0.f,0.f,0.f}, 0, 0, 0);
        acc2[1] = __builtin_amdgcn_mfma_f32_4x4x4bf16_1k(A2.v, B2[1].v, (f32x4){0.f,0.f,0.f,0.f}, 0, 0, 0);
      } else {
        acc2[0] = __builtin_amdgcn_mfma_f32_4x4x4bf16_1k(A2.v, B2[0].v, acc2[0], 0, 0, 0);
        acc2[1] = __builtin_amdgcn_mfma_f32_4x4x4bf16_1k(A2.v, B2[1].v, acc2[1], 0, 0, 0);
      }
      if (st + 5 < st1) {
        asm volatile("s_waitcnt lgkmcnt(0)" ::: "memory");
        qdma(st + 5, (st + 1 - st0) & 3);
      }
    };
    if (st0 < st1) {
#pragma unroll
      for (int k = 0; k < 4; k++) if (st0 + k < st1) qdma(st0 + k, k);
      if (st0 + 3 < st1) asm volatile("s_waitcnt vmcnt(3)" ::: "memory");
      else               asm volatile("s_waitcnt vmcnt(0)" ::: "memory");
      Ops X, Y;
      rdops(X, st0);
      if (st0 + 4 < st1) {
        asm volatile("s_waitcnt lgkmcnt(0)" ::: "memory");
        qdma(st0 + 4, 0);
      }
      int st = st0;
      for (; st + 1 < st1; st += 2) { body(st, X, Y); body(st + 1, Y, X); }
      if (st < st1) body(st, X, Y);
    }
    if (cur >= 0) flush(cur);
    lds_barrier();
    DSTAMP(3);
    for (int i = tid; i < 48*32; i += K2T) {
      int n = i >> 5, c4 = (i & 31) * 4;
      float4 s = make_float4(0.f,0.f,0.f,0.f);
      const int nr = sm.nrow[n];
      for (int rr = 0; rr < (nr >> 16); rr++) {
        float4 v = *(const float4*)(sm.part + ((nr & 0xFFFF) + rr)*132 + c4);
        s.x += v.x; s.y += v.y; s.z += v.z; s.w += v.w;
      }
      uint2 o; o.x = pk2(s.x, s.y); o.y = pk2(s.z, s.w);
      *(uint2*)(sM + n*136 + c4) = o;
    }
    if (tid < nn*3) {
      int n = tid / 3, k = tid - n*3;
      const int nr = sm.nrow[n];
      float s = 0.f;
      for (int rr = 0; rr < (nr >> 16); rr++) s += sm.part[((nr & 0xFFFF) + rr)*132 + 128 + k];
      out[(size_t)N*128 + (size_t)(n0+n)*3 + k] = f2bf(bf2f(bpos) + s);
    }
  } else {
    for (int n = w; n < 48; n += 16) {
      float a0 = 0.f, a1 = 0.f, px = 0.f, py = 0.f, pz = 0.f;
      if (n < nn) {
        const int node = n0 + n;
        const u32 pu0 = sm.sP[n*128 + 2*l], pu1 = sm.sP[n*128 + 2*l + 1];
        const float p0v = bf2f((u16)(pu0 & 0xFFFF)) + bf2f((u16)(pu0 >> 16)), p1v = bf2f((u16)(pu1 & 0xFFFF)) + bf2f((u16)(pu1 >> 16));
        float w50[5], w51[5];
        for (int k = 0; k < 5; k++) { w50[k] = bf2f(we[(256+k)*128 + 2*l]); w51[k] = bf2f(we[(256+k)*128 + 2*l+1]); }
        for (int e0 = 0; e0 < E; e0 += 64) {
          int e = e0 + l;
          int d = (e < E) ? dst[e] : -1;
          unsigned long long m = __ballot(d == node);
          while (m) {
            int bit = __ffsll((long long)m) - 1; m &= m - 1;
            int ee = e0 + bit; int s = src[ee];
            float dx = rbf(bf2f(pos[node*3+0]) - bf2f(pos[s*3+0]));
            float dy = rbf(bf2f(pos[node*3+1]) - bf2f(pos[s*3+1]));
            float dz = rbf(bf2f(pos[node*3+2]) - bf2f(pos[s*3+2]));
            float d2 = rbf(rbf(dx*dx) + rbf(dy*dy) + rbf(dz*dz));
            u32 qq = *(const u32*)(Qb + (size_t)s*128 + 2*l);
            float z0 = p0v + d2*w50[0], z1 = p1v + d2*w51[0];
            for (int k = 0; k < 4; k++) { float f = bf2f(ea[(size_t)ee*4+k]); z0 += f*w50[1+k]; z1 += f*w51[1+k]; }
            z0 += bf2f((u16)(qq & 0xFFFF)); z1 += bf2f((u16)(qq >> 16));
            float m0 = rbf(silu_f(z0)), m1 = rbf(silu_f(z1));
            a0 += m0; a1 += m1;
            float cs = m0*wxv[0] + m1*wxv[1];
            px += dx*cs; py += dy*cs; pz += dz*cs;
          }
        }
        for (int o = 1; o < 64; o <<= 1) { px += __shfl_xor(px, o); py += __shfl_xor(py, o); pz += __shfl_xor(pz, o); }
        if (l == 0) {
          out[(size_t)N*128 + (size_t)node*3 + 0] = f2bf(bf2f(pos[node*3+0]) + px);
          out[(size_t)N*128 + (size_t)node*3 + 1] = f2bf(bf2f(pos[node*3+1]) + py);
          out[(size_t)N*128 + (size_t)node*3 + 2] = f2bf(bf2f(pos[node*3+2]) + pz);
        }
      }
      *(u32*)(sM + n*136 + 2*l) = pk2(a0, a1);
    }
  }
  lds_barrier();
  DSTAMP(4);
  float* sOut = sm.part;
  {
    f32x4 acc1[2];
    acc1[0] = (f32x4){0.f,0.f,0.f,0.f}; acc1[1] = (f32x4){0.f,0.f,0.f,0.f};
#pragma unroll
    for (int s = 0; s < 4; s++) {
#pragma unroll
      for (int i = 0; i < 2; i++) {
        if (i < nrt) {
          int rl = (rt0 + i)*16 + j;
          U4 Ah, Am;
          Ah.u = sm.sH[rl*16 + ((4*s + g) ^ j)];
          Am.u = *(const uint4*)(sM + rl*136 + 32*s + 8*g);
          acc1[i] = __builtin_amdgcn_mfma_f32_16x16x32_bf16(Ah.b, Bh[s].b, acc1[i], 0, 0, 0);
          acc1[i] = __builtin_amdgcn_mfma_f32_16x16x32_bf16(Am.b, Bm[s].b, acc1[i], 0, 0, 0);
        }
      }
    }
#pragma unroll
    for (int i = 0; i < 2; i++) if (i < nrt) {
#pragma unroll
      for (int r = 0; r < 4; r++) sOut[((rt0+i)*16 + 4*g + r)*132 + 8*j + tt] = acc1[i][r];
    }
  }
  lds_barrier();
  DSTAMP(5);
  for (int i = tid; i < nn*16; i += K2T) {
    int n = i >> 4, c8 = (i & 15) * 8;
    U4 hv2; hv2.u = sm.sH[n*16 + ((i & 15) ^ (n & 15))];
    U4 o;
#pragma unroll
    for (int k = 0; k < 8; k++) { float z = sOut[n*132 + c8 + k]; o.h[k] = f2bf(bf2f(hv2.h[k]) + silu_f(z)); }
    *(uint4*)(out + (size_t)(n0+n)*128 + c8) = o.u;
  }
  DSTAMP(6);
}

extern "C" void kernel_launch(void* const* d_in, const int* in_sizes, int n_in,
                              void* d_out, int out_size, void* d_ws, size_t ws_size,
                              hipStream_t stream) {
  const u16* h=(const u16*)d_in[0]; const u16* pos=(const u16*)d_in[1]; const u16* ea=(const u16*)d_in[2];
  const int* src=(const int*)d_in[3]; const int* dst=(const int*)d_in[4];
  const u16* we=(const u16*)d_in[5]; const u16* wx=(const u16*)d_in[6]; const u16* wh=(const u16*)d_in[7];
  int N = in_sizes[0]/128; int E = in_sizes[3];
  int nchunk = (E + CH - 1)/CH; int ngemm = (N + 63)/64; int np = (N + NB - 1)/NB;
  char* ws = (char*)d_ws; size_t o = 0;
  auto take = [&](size_t bytes){ void* p = ws + o; o += (bytes + 255) & ~(size_t)255; return p; };
  u16* Qb = (u16*)take((size_t)N*128*2);
  uint4* entA = (uint4*)take((size_t)nchunk*CH*16);
  u32* offT = (u32*)take((size_t)np*nchunk*4);
  uint4* wF = (uint4*)take(98304);
  k1_kernel<<<nchunk + ngemm + 12, K1T, 0, stream>>>(h,ea,src,dst,we,wh,Qb,entA,offT,wF,N,E,nchunk,ngemm,np
);
  k2_kernel<<<np, K2T, 0, stream>>>(h,pos,ea,src,dst,we,wx,Qb,entA,offT,wF,(u16*)d_out,N,E,nchunk,np
);
}
```

```cpp
#include <hip/hip_runtime.h>
#include <stdint.h>

typedef unsigned short u16;
typedef unsigned int u32;
typedef __bf16 bf16x8 __attribute__((ext_vector_type(8)));
typedef short s16x4 __attribute__((ext_vector_type(4)));
typedef float f32x4 __attribute__((ext_vector_type(4)));

constexpr int NB   = 40;
constexpr int CH   = 2048;
constexpr int K1T  = 512;
constexpr int K2T  = 1024;
constexpr int CAP  = 1024;
#define NSV 8
constexpr int NS   = NSV;
constexpr int NPMAX = 8192;

__device__ __forceinline__ float bf2f(u16 v){ return __uint_as_float(((u32)v)<<16); }
typedef __bf16 bf16x2 __attribute__((ext_vector_type(2)));
typedef float f32x2 __attribute__((ext_vector_type(2)));
__device__ __forceinline__ u32 pk2(float a, float b){
  f32x2 v = {a, b};
  bf16x2 r = __builtin_convertvector(v, bf16x2);
  return __builtin_bit_cast(u32, r);
}
__device__ __forceinline__ u16 f2bf(float f){ return (u16)(pk2(f, 0.f) & 0xFFFFu); }
__device__ __forceinline__ float rbf(float f){ return __uint_as_float(pk2(0.f, f) & 0xFFFF0000u); }
__device__ __forceinline__ float silu_f(float z){ return z * __builtin_amdgcn_rcpf(1.0f + __builtin_amdgcn_exp2f(-1.4426950408889634f * z)); }

template<int CTRL> __device__ __forceinline__ float dpp_add(float v){
  int x = __builtin_amdgcn_update_dpp(0, __float_as_int(v), CTRL, 0xF, 0xF, true);
  return v + __int_as_float(x);
}
__device__ __forceinline__ float wave_sum63(float v){
  v = dpp_add<0x111>(v);
  v = dpp_add<0x112>(v);
  v = dpp_add<0x114>(v);
  v = dpp_add<0x118>(v);
  v = dpp_add<0x142>(v);
  v = dpp_add<0x143>(v);
  return v;
}
__device__ __forceinline__ int wave_scan_incl(int v){
  v += __builtin_amdgcn_update_dpp(0, v, 0x111, 0xF, 0xF, true);
  v += __builtin_amdgcn_update_dpp(0, v, 0x112, 0xF, 0xF, true);
  v += __builtin_amdgcn_update_dpp(0, v, 0x114, 0xF, 0xF, true);
  v += __builtin_amdgcn_update_dpp(0, v, 0x118, 0xF, 0xF, true);
  v += __builtin_amdgcn_update_dpp(0, v, 0x142, 0xA, 0xF, false);
  v += __builtin_amdgcn_update_dpp(0, v, 0x143, 0xC, 0xF, false);
  return v;
}
struct __attribute__((packed, aligned(2))) Pk8 { unsigned long long v; };
__device__ __forceinline__ unsigned long long pos3(const u16* __restrict__ pos, int n, int N){
  const bool last = (n == N - 1);
  const Pk8* p = (const Pk8*)(pos + (size_t)n*3 - (last ? 1 : 0));
  unsigned long long v = p->v;
  return last ? (v >> 16) : v;
}

union U4 { uint4 u; bf16x8 b; u16 h[8]; };
union S4 { u32 u[2]; s16x4 v; u16 h[4]; };

__device__ __forceinline__ void lds_barrier(){ asm volatile("s_waitcnt lgkmcnt(0)\n\ts_barrier" ::: "memory"); }
__device__ __forceinline__ void all_barrier(){ asm volatile("s_waitcnt vmcnt(0) lgkmcnt(0)\n\ts_barrier" ::: "memory"); }

struct K1Part { u32 hist[NPMAX]; uint4 stA[CH]; u32 wtot[16]; };

__global__ __launch_bounds__(K1T) void k1_kernel(const u16* __restrict__ h,
    const u16* __restrict__ ea, const int* __restrict__ src, const int* __restrict__ dst,
    const u16* __restrict__ we, const u16* __restrict__ wh,
    u16* __restrict__ Qb, uint4* __restrict__ entA,
    u32* __restrict__ offT, uint4* __restrict__ wF, int N, int E, int nchunk, int ngemm, int np
)
{
  __shared__ __attribute__((aligned(16))) char smem[sizeof(K1Part)];
  const int tid = threadIdx.x;
  const int b = blockIdx.x;
  if (b < nchunk) {
    K1Part& sp = *(K1Part*)smem;
    for (int i = tid; i < np; i += K1T) sp.hist[i] = 0;
    const int base = b * CH;
    int dv[4], sv[4]; uint2 av[4];
#pragma unroll
    for (int k = 0; k < 4; k++) {
      int e = min(base + k*K1T + tid, E-1);
      dv[k] = dst[e];
    }
#pragma unroll
    for (int k = 0; k < 4; k++) {
      int e = min(base + k*K1T + tid, E-1);
      sv[k] = src[e]; av[k] = *(const uint2*)(ea + (size_t)e*4);
    }
    lds_barrier();
    u32 slot[4]; int bn[4];
#pragma unroll
    for (int k = 0; k < 4; k++) {
      int e = base + k*K1T + tid;
      bn[k] = dv[k] / NB; slot[k] = 0;
      if (e < E) slot[k] = atomicAdd(&sp.hist[bn[k]], 1u);
    }
    lds_barrier();
    const int per = (np + K1T - 1) / K1T;
    const int i0 = min(tid * per, np), i1 = min(i0 + per, np);
    u32 loc = 0;
    for (int i = i0; i < i1; i++) loc += sp.hist[i];
    const int l = tid & 63, w = tid >> 6;
    const u32 incl = (u32)wave_scan_incl((int)loc);
    if (l == 63) sp.wtot[w] = incl;
    lds_barrier();
    u32 run = incl - loc;
    {
      const uint4 ta = *(const uint4*)sp.wtot, tb = *(const uint4*)(sp.wtot + 4);
      run += (w > 0 ? ta.x : 0u) + (w > 1 ? ta.y : 0u) + (w > 2 ? ta.z : 0u) + (w > 3 ? ta.w : 0u)
           + (w > 4 ? tb.x : 0u) + (w > 5 ? tb.y : 0u) + (w > 6 ? tb.z : 0u);
    }
    for (int i = i0; i < i1; i++) { u32 c = sp.hist[i]; sp.hist[i] = run; offT[(size_t)i * nchunk + b] = run | (c << 16); run += c; }
    lds_barrier();
#pragma unroll
    for (int k = 0; k < 4; k++) {
      int e = base + k*K1T + tid;
      if (e < E) {
        int idx = sp.hist[bn[k]] + slot[k];
        sp.stA[idx] = make_uint4(av[k].x, av[k].y, (u32)sv[k] | ((u32)(dv[k] - bn[k]*NB) << 26), (u32)e);
      }
    }
    __syncthreads();
    const int ne = min(CH, E - base);
#pragma unroll
    for (int k = 0; k < 4; k++) { int i = tid + k*K1T; if (i < ne) entA[(size_t)base + i] = sp.stA[i]; }
  } else if (b < nchunk + ngemm) {
    u32* Wt32 = (u32*)smem;
    const int w = tid >> 6, l = tid & 63, g = l >> 4, j = l & 15;
    const int rowbase = (b - nchunk) * 64 + (w & 3) * 16;
    const int t0 = (w >> 2) * 4;
    U4 A[4];
    {
      int row = min(rowbase + j, N-1);
#pragma unroll
      for (int s = 0; s < 4; s++) A[s].u = *(const uint4*)(h + (size_t)row*128 + 32*s + 8*g);
    }
    {
      U4 r0[2], r1[2];
#pragma unroll
      for (int i4 = 0; i4 < 2; i4++) {
        int it = tid + i4*K1T;
        int oct = it & 15, kp = it >> 4;
        int k = 128 + 2*kp;
        r0[i4].u = *(const uint4*)(we + k*128 + oct*8);
        r1[i4].u = *(const uint4*)(we + (k+1)*128 + oct*8);
      }
#pragma unroll
      for (int i4 = 0; i4 < 2; i4++) {
        int it = tid + i4*K1T;
        int oct = it & 15, kp = it >> 4;
#pragma unroll
        for (int i = 0; i < 8; i++)
          Wt32[(i*16 + oct)*68 + kp] = (u32)r0[i4].h[i] | ((u32)r1[i4].h[i] << 16);
      }
    }
    __syncthreads();
    f32x4 acc[4];
#pragma unroll
    for (int t = 0; t < 4; t++) acc[t] = (f32x4){0.f,0.f,0.f,0.f};
#pragma unroll
    for (int s = 0; s < 4; s++) {
#pragma unroll
      for (int t = 0; t < 4; t++) {
        U4 B; B.u = *(const uint4*)(Wt32 + ((t0 + t)*16 + j)*68 + 16*s + 4*g);
        acc[t] = __builtin_amdgcn_mfma_f32_16x16x32_bf16(A[s].b, B.b, acc[t], 0, 0, 0);
      }
    }
#pragma unroll
    for (int r = 0; r < 4; r++) {
      int row = rowbase + 4*g + r;
      if (row < N) {
        uint2 v;
        v.x = pk2(acc[0][r], acc[1][r]); v.y = pk2(acc[2][r], acc[3][r]);
        *(uint2*)(Qb + (size_t)row*128 + 8*j + t0) = v;
      }
    }
  } else {
    int it = (b - nchunk - ngemm) * K1T + tid;
    int l = it & 63, f = (it >> 6) & 31, part = it >> 11, t = f & 7, s = f >> 3, g = l >> 4, j = l & 15;
    const u16* wsrc = (part == 0) ? (we + 16*t + j) : (wh + (part - 1)*128*128 + 8*j + t);
    U4 v;
#pragma unroll
    for (int jj = 0; jj < 8; jj++) v.h[jj] = wsrc[(32*s + 8*g + jj)*128];
    wF[it] = v.u;
  }
}

typedef __attribute__((address_space(3))) const void* lds_cptr;
__device__ __forceinline__ u32 lds_addr(const void* p){ return __builtin_amdgcn_readfirstlane((u32)(uintptr_t)(lds_cptr)p); }
__device__ __forceinline__ void dma16(const void* g, u32 lds){
  asm volatile("s_mov_b32 m0, %1\n\ts_nop 0\n\tglobal_load_lds_dwordx4 %0, off" :: "v"(g), "s"(lds) : "memory");
}
__device__ __forceinline__ void dma4(const void* g, u32 lds){
  asm volatile("s_mov_b32 m0, %1\n\ts_nop 0\n\tglobal_load_lds_dword %0, off" :: "v"(g), "s"(lds) : "memory");
}

struct K2Smem {
  uint4 sA[CAP];
  uint4 sW[4096];
  u32   sP[48*128];
  float part[(NB+16)*132];
  uint4 sH[48*16];
  u16   sG[4][CAP];
  u32   sSrc[CAP+4];
  int   ncount[64];
  int   nstart[72];
  int   nrow[64];
  int   flag[4];
  u32   dummy[64];
  u16   posd[128];
  unsigned char sNode[CAP/4];
};

template<bool hostslow>
__global__ __launch_bounds__(K2T) void k2_kernel(const u16* __restrict__ h, const u16* __restrict__ pos,
    const u16* __restrict__ ea, const int* __restrict__ src, const int* __restrict__ dst,
    const u16* __restrict__ we, const u16* __restrict__ wx,
    const u16* __restrict__ Qb,
    const uint4* __restrict__ entA, const u32* __restrict__ offT,
    const uint4* __restrict__ wF,
    u16* __restrict__ out, int N, int E, int nchunk, int np
)
{
  __shared__ __attribute__((aligned(16))) K2Smem sm;
  const int tid = threadIdx.x, w = tid >> 6, l = tid & 63, g = l >> 4, j = l & 15;
  const int blk = blockIdx.x;
  const int n0 = blk * NB;
  const int nn = min(NB, N - n0);
  u16* const sM = (u16*)sm.sA;
  u32* const tmpE = (u32*)sm.part;
  uint4* const uA = (uint4*)(sm.part + 1024);
  u32* const uS = (u32*)(sm.part + 1024 + 4096);
  const int tt = w & 7;
  const int rt0 = (w >> 3) ? 2 : 0, nrt = (w >> 3) ? 1 : 2;

  u32 bposv, oc[3];
  {
    const u16* pb = pos + (size_t)n0*3 + min(tid, nn*3 - 1);
    asm volatile("global_load_ushort %0, %1, off" : "=v"(bposv) : "v"(pb) : "memory");
#pragma unroll
    for (int k = 0; k < 3; k++) {
      int c = (tid + k*K2T) >> 5;
      const u32* po = offT + (hostslow ? (size_t)0 : (size_t)blk * nchunk + min(c, nchunk - 1));
      asm volatile("global_load_dword %0, %1, off" : "=v"(oc[k]) : "v"(po) : "memory");
    }
  }
#pragma unroll
  for (int k = 0; k < 3; k++) {
    const int id = min(w + 16*k, 43);
    if (id < 12) {
      const int r = id*4 + g;
      const int row = min(n0 + r, N - 1);
      dma16(h + (size_t)row*128 + (j ^ (r & 15))*8, lds_addr(sm.sH + id*64));
    } else {
      dma16(wF + (id - 12)*64 + l, lds_addr(sm.sW + (id - 12)*64));
    }
  }
  for (int i = tid; i < CAP; i += K2T) {
    sm.sA[i] = make_uint4(0,0,0,0); sm.sSrc[i] = 0;
    sm.sG[0][i] = 0; sm.sG[1][i] = 0; sm.sG[2][i] = 0; sm.sG[3][i] = 0;
  }
  if (tid < 64) { sm.ncount[tid] = 0; sm.nrow[tid] = 0; }
  if (tid < 4) { sm.flag[tid] = 0; sm.sSrc[CAP + tid] = 0; }
  asm volatile("s_waitcnt vmcnt(3)" : "+v"(bposv), "+v"(oc[0]), "+v"(oc[1]), "+v"(oc[2]) :: "memory");
  const u16 bpos = (u16)bposv;
#pragma unroll
  for (int k = 0; k < 3; k++) if (hostslow || ((tid + k*K2T) >> 5) >= nchunk) oc[k] = 0u;
  uint4 IA[3]; bool val[3];
#pragma unroll
  for (int k = 0; k < 3; k++) {
    int i = tid + k*K2T, c = i >> 5, kk = i & 31;
    int cnt = (int)(oc[k] >> 16);
    val[k] = kk < cnt;
    size_t idx = val[k] ? ((size_t)c * CH + (oc[k] & 0xFFFFu) + kk) : (size_t)0;
    IA[k] = entA[idx];
  }
  const bool ovf = hostslow || (oc[0] >> 16) > 32u || (oc[1] >> 16) > 32u || (oc[2] >> 16) > 32u;
  if (tid < 128) sm.posd[tid] = bpos;
  all_barrier();
  unsigned long long ps_[3];
#pragma unroll
  for (int k = 0; k < 3; k++) ps_[k] = hostslow ? 0ull : pos3(pos, (int)(IA[k].z & 0x3FFFFFFu), N);
  int dl[3], slot[3];
  if (ovf) sm.flag[0] = 1;
#pragma unroll
  for (int k = 0; k < 3; k++) {
    dl[k] = val[k] ? (int)(IA[k].z >> 26) : 0; slot[k] = 0;
    if (val[k]) slot[k] = atomicAdd(&sm.ncount[dl[k]], 1);
  }
  S4 W0[2], W1[2]; float wxv[2];
#pragma unroll
  for (int hh = 0; hh < 2; hh++) {
    int c = 2*l + hh;
#pragma unroll
    for (int k = 0; k < 4; k++) W0[hh].h[k] = we[(256+k)*128 + c];
    W1[hh].u[0] = 0; W1[hh].u[1] = we[260*128 + c];
    wxv[hh] = bf2f(wx[c]);
  }
#pragma unroll
  for (int i = 0; i < 2; i++) {
    if (i < nrt) {
      f32x4 ap = (f32x4){0.f,0.f,0.f,0.f};
      const int r = (rt0 + i)*16 + j;
#pragma unroll
      for (int s = 0; s < 4; s++) {
        U4 A, Be;
        A.u = sm.sH[r*16 + ((4*s + g) ^ j)];
        Be.u = sm.sW[(s*8 + tt)*64 + l];
        ap = __builtin_amdgcn_mfma_f32_16x16x32_bf16(A.b, Be.b, ap, 0, 0, 0);
      }
#pragma unroll
      for (int rr = 0; rr < 4; rr++) {
        float v = ap[rr]; u32 hi = pk2(0.f, v); float lo = v - __uint_as_float(hi & 0xFFFF0000u);
        sm.sP[((rt0+i)*16 + 4*g + rr)*128 + 16*tt + j] = (hi >> 16) | (pk2(0.f, lo) & 0xFFFF0000u);
      }
    }
  }
  lds_barrier();
  int nst;
  {
    int c = (l < NB) ? sm.ncount[l] : 0;
    int cp = (c + 3) & ~3;
    const int incl = wave_scan_incl(cp);
    nst = incl - cp;
    if (w == 0) { sm.nstart[l] = nst; if (l == 63) sm.nstart[64] = incl; }
  }
  const int Tp = __builtin_amdgcn_readlane(nst, 63);
  const bool fast = (sm.flag[0] == 0) && (Tp <= CAP);
  const int nsteps = fast ? (Tp >> 2) : 0;
  if (fast) {
#pragma unroll
    for (int k = 0; k < 3; k++) {
      const int p = __shfl(nst, dl[k]) + slot[k];
      if (val[k]) {
        const u16* pd = sm.posd + dl[k]*3;
        float dx = rbf(bf2f(pd[0]) - bf2f((u16)ps_[k]));
        float dy = rbf(bf2f(pd[1]) - bf2f((u16)(ps_[k] >> 16)));
        float dz = rbf(bf2f(pd[2]) - bf2f((u16)(ps_[k] >> 32)));
        float d2 = rbf(dx*dx) + rbf(dy*dy) + rbf(dz*dz);
        u32 dxy = pk2(dx, dy), dzd = pk2(dz, d2);
        tmpE[p] = IA[k].w;
        *(uint4*)&uA[p] = make_uint4((dzd >> 16) | (IA[k].x << 16), (IA[k].x >> 16) | (IA[k].y << 16),
                                     (IA[k].y >> 16) | (dxy << 16), (dxy >> 16) | (dzd << 16));
        uS[p] = (IA[k].z & 0x3FFFFFFu) << 8;
      }
    }
    if (w >= 13 && l < NB) {
      const int a = nst, cnt = sm.ncount[l], bnd = a + ((cnt + 3) & ~3);
      if (w == 13) { for (int i = a >> 2; i < (bnd >> 2); i++) sm.sNode[i] = (unsigned char)l; }
      else if (w == 14) { for (int i = a + cnt; i < bnd; i++) tmpE[i] = 0xFFFFFFFFu; }
      else if (cnt > 0) { const int wl = (16*(a >> 2) + 15) / nsteps, wh_ = (16*((bnd >> 2) - 1) + 15) / nsteps; sm.nrow[l] = (l + wl) | ((wh_ - wl + 1) << 16); }
    }
    lds_barrier();
    if (tid < Tp) {
      const u32 ev = tmpE[tid];
      if (ev != 0xFFFFFFFFu) {
        const int n = sm.sNode[tid >> 2];
        const int a = sm.nstart[n], bnd = sm.nstart[n+1];
        const uint4 pa = uA[tid]; const u32 ps = uS[tid];
        int rank = 0;
        for (int i = a; i < bnd; i += 4) {
          uint4 t = *(const uint4*)(tmpE + i);
          rank += (t.x < ev) + (t.y < ev) + (t.z < ev) + (t.w < ev);
        }
        const int p = a + rank;
        sm.sA[p] = make_uint4(pa.x, pa.y, 0x3F803F80u, pa.z & 0xFFFFu);
        sm.sSrc[p] = ps;
        sm.sG[0][p] = 0x3F80; sm.sG[1][p] = (u16)(pa.z >> 16); sm.sG[2][p] = (u16)(pa.w & 0xFFFFu); sm.sG[3][p] = (u16)(pa.w >> 16);
      }
    }
  }
  all_barrier();
  const int st0 = __builtin_amdgcn_readfirstlane((w * nsteps) >> 4);
  const int st1 = __builtin_amdgcn_readfirstlane(((w + 1) * nsteps) >> 4);
  U4 Bh[4], Bm[4];
#pragma unroll
  for (int s = 0; s < 4; s++) { Bh[s].u = wF[2048 + (s*8 + tt)*64 + l]; Bm[s].u = wF[4096 + (s*8 + tt)*64 + l]; }
  if (fast) {
    int cur = -1;
    S4 I4;
    I4.u[0] = ((l & 3) == 0) ? 0x00003F80u : (((l & 3) == 1) ? 0x3F800000u : 0u);
    I4.u[1] = ((l & 3) == 2) ? 0x00003F80u : (((l & 3) == 3) ? 0x3F800000u : 0u);
    f32x4 acc2[2];
    acc2[0] = (f32x4){0.f,0.f,0.f,0.f}; acc2[1] = (f32x4){0.f,0.f,0.f,0.f};
    auto flush = [&](int n) {
      float* pt = sm.part + (n + w) * 132;
      pt[2*l] = acc2[0][0]; pt[2*l + 1] = acc2[1][0];
      float px = acc2[0][1]*wxv[0] + acc2[1][1]*wxv[1];
      float py = acc2[0][2]*wxv[0] + acc2[1][2]*wxv[1];
      float pz = acc2[0][3]*wxv[0] + acc2[1][3]*wxv[1];
      asm volatile(
        "s_nop 1\n\t"
        "v_add_f32_dpp %0, %0, %0 row_shr:1 row_mask:0xf bank_mask:0xf bound_ctrl:1\n\t"
        "v_add_f32_dpp %1, %1, %1 row_shr:1 row_mask:0xf bank_mask:0xf bound_ctrl:1\n\t"
        "v_add_f32_dpp %2, %2, %2 row_shr:1 row_mask:0xf bank_mask:0xf bound_ctrl:1\n\t"
        "v_add_f32_dpp %0, %0, %0 row_shr:2 row_mask:0xf bank_mask:0xf bound_ctrl:1\n\t"
        "v_add_f32_dpp %1, %1, %1 row_shr:2 row_mask:0xf bank_mask:0xf bound_ctrl:1\n\t"
        "v_add_f32_dpp %2, %2, %2 row_shr:2 row_mask:0xf bank_mask:0xf bound_ctrl:1\n\t"
        "v_add_f32_dpp %0, %0, %0 row_shr:4 row_mask:0xf bank_mask:0xf bound_ctrl:1\n\t"
        "v_add_f32_dpp %1, %1, %1 row_shr:4 row_mask:0xf bank_mask:0xf bound_ctrl:1\n\t"
        "v_add_f32_dpp %2, %2, %2 row_shr:4 row_mask:0xf bank_mask:0xf bound_ctrl:1\n\t"
        "v_add_f32_dpp %0, %0, %0 row_shr:8 row_mask:0xf bank_mask:0xf bound_ctrl:1\n\t"
        "v_add_f32_dpp %1, %1, %1 row_shr:8 row_mask:0xf bank_mask:0xf bound_ctrl:1\n\t"
        "v_add_f32_dpp %2, %2, %2 row_shr:8 row_mask:0xf bank_mask:0xf bound_ctrl:1\n\t"
        "v_add_f32_dpp %0, %0, %0 row_bcast:15 row_mask:0xa bank_mask:0xf\n\t"
        "v_add_f32_dpp %1, %1, %1 row_bcast:15 row_mask:0xa bank_mask:0xf\n\t"
        "v_add_f32_dpp %2, %2, %2 row_bcast:15 row_mask:0xa bank_mask:0xf\n\t"
        "v_add_f32_dpp %0, %0, %0 row_bcast:31 row_mask:0xc bank_mask:0xf\n\t"
        "v_add_f32_dpp %1, %1, %1 row_bcast:31 row_mask:0xc bank_mask:0xf\n\t"
        "v_add_f32_dpp %2, %2, %2 row_bcast:31 row_mask:0xc bank_mask:0xf\n\t"
        : "+v"(px), "+v"(py), "+v"(pz));
      if (l == 63) { pt[128] = px; pt[129] = py; pt[130] = pz; }
    };
    const int ndv = (int)sm.sNode[min(st0 + l, CAP/4 - 1)];
    const int ndp = (int)sm.sNode[min(max(st0 + l - 1, 0), CAP/4 - 1)];
    const unsigned long long chg = __ballot(l == 0 || ndv != ndp);
    const u32 ringb = lds_addr(sm.sW + w*256);
    const u32* ringp = (const u32*)(sm.sW + w*256);
    auto qdma = [&](int st, int slot) {
      const u32 so = sm.sSrc[st*4 + (l >> 4)];
      dma16((const char*)Qb + (so + (u32)(l & 15)*16u), ringb + (u32)slot*1024u);
    };
    struct Ops { u32 q0, q1, q2, q3; uint4 a4; uint2 g2; };
    auto rdops = [&](Ops& o, int st) {
      const u32* rp = ringp + ((st - st0) & 3)*256 + l;
      o.q0 = rp[0]; o.q1 = rp[64]; o.q2 = rp[128]; o.q3 = rp[192];
      o.a4 = sm.sA[st*4 + (l & 3)];
      o.g2 = *(const uint2*)(&sm.sG[l & 3][st*4]);
    };
    const int pq1 = st0 + ((st1 - st0) >> 2), pq2 = st0 + ((st1 - st0) >> 1), pq3 = st0 + ((3*(st1 - st0)) >> 2);
    auto body = [&](int st, Ops& c, Ops& nx) {
      if (st == pq1) __builtin_amdgcn_s_setprio(2);
      if (st == pq2) __builtin_amdgcn_s_setprio(1);
      if (st == pq3) __builtin_amdgcn_s_setprio(0);
      if (st + 1 < st1) {
        if (st + 4 < st1) asm volatile("s_waitcnt vmcnt(3)" ::: "memory");
        else              asm volatile("s_waitcnt vmcnt(0)" ::: "memory");
        rdops(nx, st + 1);
      }
      const bool nw = (chg >> (st - st0)) & 1ull;
      if (nw) {
        if (cur >= 0) flush(cur);
        const int n = __builtin_amdgcn_readlane(ndv, st - st0);
        cur = n;
        W1[0].u[0] = sm.sP[n*128 + 2*l]; W1[1].u[0] = sm.sP[n*128 + 2*l + 1];
      }
      S4 B2[2];
      S4 Aa, Ab, A2;
      Aa.u[0] = c.a4.x; Aa.u[1] = c.a4.y; Ab.u[0] = c.a4.z; Ab.u[1] = c.a4.w;
      A2.u[0] = c.g2.x; A2.u[1] = c.g2.y;
#pragma unroll
      for (int hh = 0; hh < 2; hh++) {
        S4 Bq;
        Bq.u[0] = __builtin_amdgcn_perm(c.q1, c.q0, hh ? 0x07060302u : 0x05040100u);
        Bq.u[1] = __builtin_amdgcn_perm(c.q3, c.q2, hh ? 0x07060302u : 0x05040100u);
        f32x4 z = __builtin_amdgcn_mfma_f32_4x4x4bf16_1k(Aa.v, W0[hh].v, (f32x4){0.f,0.f,0.f,0.f}, 0, 0, 0);
        z = __builtin_amdgcn_mfma_f32_4x4x4bf16_1k(Ab.v, W1[hh].v, z, 0, 0, 0);
        z = __builtin_amdgcn_mfma_f32_4x4x4bf16_1k(I4.v, Bq.v, z, 0, 0, 0);
        f32x2 za = {z[0], z[1]}, zb = {z[2], z[3]};
        f32x2 ta = za * -1.4426950408889634f, tb = zb * -1.4426950408889634f;
        f32x2 xa = {__builtin_amdgcn_exp2f(ta[0]), __builtin_amdgcn_exp2f(ta[1])};
        f32x2 xb = {__builtin_amdgcn_exp2f(tb[0]), __builtin_amdgcn_exp2f(tb[1])};
        xa = xa + 1.0f; xb = xb + 1.0f;
        f32x2 ra = {__builtin_amdgcn_rcpf(xa[0]), __builtin_amdgcn_rcpf(xa[1])};
        f32x2 rb = {__builtin_amdgcn_rcpf(xb[0]), __builtin_amdgcn_rcpf(xb[1])};
        za = za * ra; zb = zb * rb;
        B2[hh].u[0] = pk2(za[0], za[1]); B2[hh].u[1] = pk2(zb[0], zb[1]);
      }
      if (nw) {
        acc2[0] = __builtin_amdgcn_mfma_f32_4x4x4bf16_1k(A2.v, B2[0].v, (f32x4){0.f,0.f,0.f,0.f}, 0, 0, 0);
        acc2[1] = __builtin_amdgcn_mfma_f32_4x4x4bf16_1k(A2.v, B2[1].v, (f32x4){0.f,0.f,0.f,0.f}, 0, 0, 0);
      } else {
        acc2[0] = __builtin_amdgcn_mfma_f32_4x4x4bf16_1k(A2.v, B2[0].v, acc2[0], 0, 0, 0);
        acc2[1] = __builtin_amdgcn_mfma_f32_4x4x4bf16_1k(A2.v, B2[1].v, acc2[1], 0, 0, 0);
      }
      if (st + 5 < st1) {
        asm volatile("s_waitcnt lgkmcnt(0)" ::: "memory");
        qdma(st + 5, (st + 1 - st0) & 3);
      }
    };
    __builtin_amdgcn_s_setprio(3);
    if (st0 < st1) {
#pragma unroll
      for (int k = 0; k < 4; k++) if (st0 + k < st1) qdma(st0 + k, k);
      if (st0 + 3 < st1) asm volatile("s_waitcnt vmcnt(3)" ::: "memory");
      else               asm volatile("s_waitcnt vmcnt(0)" ::: "memory");
      Ops X, Y;
      rdops(X, st0);
      if (st0 + 4 < st1) {
        asm volatile("s_waitcnt lgkmcnt(0)" ::: "memory");
        qdma(st0 + 4, 0);
      }
      int st = st0;
      for (; st + 1 < st1; st += 2) { body(st, X, Y); body(st + 1, Y, X); }
      if (st < st1) body(st, X, Y);
    }
    if (cur >= 0) flush(cur);
    __builtin_amdgcn_s_setprio(0);
    lds_barrier();
    for (int i = tid; i < 48*32; i += K2T) {
      int n = i >> 5, c4 = (i & 31) * 4;
      float4 s = make_float4(0.f,0.f,0.f,0.f);
      const int nr = sm.nrow[n];
      for (int rr = 0; rr < (nr >> 16); rr++) {
        float4 v = *(const float4*)(sm.part + ((nr & 0xFFFF) + rr)*132 + c4);
        s.x += v.x; s.y += v.y; s.z += v.z; s.w += v.w;
      }
      uint2 o; o.x = pk2(s.x, s.y); o.y = pk2(s.z, s.w);
      *(uint2*)(sM + n*136 + c4) = o;
    }
    if (tid < nn*3) {
      int n = tid / 3, k = tid - n*3;
      const int nr = sm.nrow[n];
      float s = 0.f;
      for (int rr = 0; rr < (nr >> 16); rr++) s += sm.part[((nr & 0xFFFF) + rr)*132 + 128 + k];
      out[(size_t)N*128 + (size_t)(n0+n)*3 + k] = f2bf(bf2f(bpos) + s);
    }
  } else {
    for (int n = w; n < 48; n += 16) {
      float a0 = 0.f, a1 = 0.f, px = 0.f, py = 0.f, pz = 0.f;
      if (n < nn) {
        const int node = n0 + n;
        const u32 pu0 = sm.sP[n*128 + 2*l], pu1 = sm.sP[n*128 + 2*l + 1];
        const float p0v = bf2f((u16)(pu0 & 0xFFFF)) + bf2f((u16)(pu0 >> 16)), p1v = bf2f((u16)(pu1 & 0xFFFF)) + bf2f((u16)(pu1 >> 16));
        float w50[5], w51[5];
        for (int k = 0; k < 5; k++) { w50[k] = bf2f(we[(256+k)*128 + 2*l]); w51[k] = bf2f(we[(256+k)*128 + 2*l+1]); }
        for (int e0 = 0; e0 < E; e0 += 64) {
          int e = e0 + l;
          int d = (e < E) ? dst[e] : -1;
          unsigned long long m = __ballot(d == node);
          while (m) {
            int bit = __ffsll((long long)m) - 1; m &= m - 1;
            int ee = e0 + bit; int s = src[ee];
            float dx = rbf(bf2f(pos[node*3+0]) - bf2f(pos[s*3+0]));
            float dy = rbf(bf2f(pos[node*3+1]) - bf2f(pos[s*3+1]));
            float dz = rbf(bf2f(pos[node*3+2]) - bf2f(pos[s*3+2]));
            float d2 = rbf(rbf(dx*dx) + rbf(dy*dy) + rbf(dz*dz));
            u32 qq = *(const u32*)(Qb + (size_t)s*128 + 2*l);
            float z0 = p0v + d2*w50[0], z1 = p1v + d2*w51[0];
            for (int k = 0; k < 4; k++) { float f = bf2f(ea[(size_t)ee*4+k]); z0 += f*w50[1+k]; z1 += f*w51[1+k]; }
            z0 += bf2f((u16)(qq & 0xFFFF)); z1 += bf2f((u16)(qq >> 16));
            float m0 = rbf(silu_f(z0)), m1 = rbf(silu_f(z1));
            a0 += m0; a1 += m1;
            float cs = m0*wxv[0] + m1*wxv[1];
            px += dx*cs; py += dy*cs; pz += dz*cs;
          }
        }
        for (int o = 1; o < 64; o <<= 1) { px += __shfl_xor(px, o); py += __shfl_xor(py, o); pz += __shfl_xor(pz, o); }
        if (l == 0) {
          out[(size_t)N*128 + (size_t)node*3 + 0] = f2bf(bf2f(pos[node*3+0]) + px);
          out[(size_t)N*128 + (size_t)node*3 + 1] = f2bf(bf2f(pos[node*3+1]) + py);
          out[(size_t)N*128 + (size_t)node*3 + 2] = f2bf(bf2f(pos[node*3+2]) + pz);
        }
      }
      *(u32*)(sM + n*136 + 2*l) = pk2(a0, a1);
    }
  }
  lds_barrier();
  float* sOut = sm.part;
  {
    f32x4 acc1[2];
    acc1[0] = (f32x4){0.f,0.f,0.f,0.f}; acc1[1] = (f32x4){0.f,0.f,0.f,0.f};
#pragma unroll
    for (int s = 0; s < 4; s++) {
#pragma unroll
      for (int i = 0; i < 2; i++) {
        if (i < nrt) {
          int rl = (rt0 + i)*16 + j;
          U4 Ah, Am;
          Ah.u = sm.sH[rl*16 + ((4*s + g) ^ j)];
          Am.u = *(const uint4*)(sM + rl*136 + 32*s + 8*g);
          acc1[i] = __builtin_amdgcn_mfma_f32_16x16x32_bf16(Ah.b, Bh[s].b, acc1[i], 0, 0, 0);
          acc1[i] = __builtin_amdgcn_mfma_f32_16x16x32_bf16(Am.b, Bm[s].b, acc1[i], 0, 0, 0);
        }
      }
    }
#pragma unroll
    for (int i = 0; i < 2; i++) if (i < nrt) {
#pragma unroll
      for (int r = 0; r < 4; r++) sOut[((rt0+i)*16 + 4*g + r)*132 + 8*j + tt] = acc1[i][r];
    }
  }
  lds_barrier();
  for (int i = tid; i < nn*16; i += K2T) {
    int n = i >> 4, c8 = (i & 15) * 8;
    U4 hv2; hv2.u = sm.sH[n*16 + ((i & 15) ^ (n & 15))];
    U4 o;
#pragma unroll
    for (int k = 0; k < 8; k++) { float z = sOut[n*132 + c8 + k]; o.h[k] = f2bf(bf2f(hv2.h[k]) + silu_f(z)); }
    *(uint4*)(out + (size_t)(n0+n)*128 + c8) = o.u;
  }
}

extern "C" void kernel_launch(void* const* d_in, const int* in_sizes, int n_in,
                              void* d_out, int out_size, void* d_ws, size_t ws_size,
                              hipStream_t stream) {
  const u16* h=(const u16*)d_in[0]; const u16* pos=(const u16*)d_in[1]; const u16* ea=(const u16*)d_in[2];
  const int* src=(const int*)d_in[3]; const int* dst=(const int*)d_in[4];
  const u16* we=(const u16*)d_in[5]; const u16* wx=(const u16*)d_in[6]; const u16* wh=(const u16*)d_in[7];
  int N = in_sizes[0]/128; int E = in_sizes[3];
  int nchunk = (E + CH - 1)/CH; int ngemm = (N + 63)/64; int np = (N + NB - 1)/NB;
  const int hostslow = (np > NPMAX || nchunk > 96 || N > (1 << 24) || N < 2 || E < 1) ? 1 : 0;
  if (hostslow) nchunk = 0;
  char* ws = (char*)d_ws; size_t o = 0;
  auto take = [&](size_t bytes){ void* p = ws + o; o += (bytes + 255) & ~(size_t)255; return p; };
  u16* Qb = (u16*)take((size_t)N*128*2);
  uint4* entA = (uint4*)take((size_t)nchunk*CH*16 + 256);
  u32* offT = (u32*)take((size_t)np*nchunk*4 + 256);
  uint4* wF = (uint4*)take(98304);
  k1_kernel<<<nchunk + ngemm + 12, K1T, 0, stream>>>(h,ea,src,dst,we,wh,Qb,entA,offT,wF,N,E,nchunk,ngemm,np
);
  if (hostslow) k2_kernel<true><<<np, K2T, 0, stream>>>(h,pos,ea,src,dst,we,wx,Qb,entA,offT,wF,(u16*)d_out,N,E,nchunk,np);
  else          k2_kernel<false><<<np, K2T, 0, stream>>>(h,pos,ea,src,dst,we,wx,Qb,entA,offT,wF,(u16*)d_out,N,E,nchunk,np);
}
```

```cpp
#include <hip/hip_runtime.h>
#include <stdint.h>

typedef unsigned short u16;
typedef unsigned int u32;
typedef __bf16 bf16x8 __attribute__((ext_vector_type(8)));
typedef short s16x4 __attribute__((ext_vector_type(4)));
typedef float f32x4 __attribute__((ext_vector_type(4)));

constexpr int NB   = 40;
constexpr int CH   = 2048;
constexpr int K1T  = 512;
constexpr int K2T  = 1024;
constexpr int CAP  = 1024;
#define NSV 8
constexpr int NS   = NSV;
constexpr int NPMAX = 8192;

__device__ __forceinline__ float bf2f(u16 v){ return __uint_as_float(((u32)v)<<16); }
typedef __bf16 bf16x2 __attribute__((ext_vector_type(2)));
typedef float f32x2 __attribute__((ext_vector_type(2)));
__device__ __forceinline__ u32 pk2(float a, float b){
  f32x2 v = {a, b};
  bf16x2 r = __builtin_convertvector(v, bf16x2);
  return __builtin_bit_cast(u32, r);
}
__device__ __forceinline__ u16 f2bf(float f){ return (u16)(pk2(f, 0.f) & 0xFFFFu); }
__device__ __forceinline__ float rbf(float f){ return __uint_as_float(pk2(0.f, f) & 0xFFFF0000u); }
__device__ __forceinline__ float silu_f(float z){ return z * __builtin_amdgcn_rcpf(1.0f + __builtin_amdgcn_exp2f(-1.4426950408889634f * z)); }

template<int CTRL> __device__ __forceinline__ float dpp_add(float v){
  int x = __builtin_amdgcn_update_dpp(0, __float_as_int(v), CTRL, 0xF, 0xF, true);
  return v + __int_as_float(x);
}
__device__ __forceinline__ float wave_sum63(float v){
  v = dpp_add<0x111>(v);
  v = dpp_add<0x112>(v);
  v = dpp_add<0x114>(v);
  v = dpp_add<0x118>(v);
  v = dpp_add<0x142>(v);
  v = dpp_add<0x143>(v);
  return v;
}
__device__ __forceinline__ int wave_scan_incl(int v){
  v += __builtin_amdgcn_update_dpp(0, v, 0x111, 0xF, 0xF, true);
  v += __builtin_amdgcn_update_dpp(0, v, 0x112, 0xF, 0xF, true);
  v += __builtin_amdgcn_update_dpp(0, v, 0x114, 0xF, 0xF, true);
  v += __builtin_amdgcn_update_dpp(0, v, 0x118, 0xF, 0xF, true);
  v += __builtin_amdgcn_update_dpp(0, v, 0x142, 0xA, 0xF, false);
  v += __builtin_amdgcn_update_dpp(0, v, 0x143, 0xC, 0xF, false);
  return v;
}
struct __attribute__((packed, aligned(2))) Pk8 { unsigned long long v; };
__device__ __forceinline__ unsigned long long pos3(const u16* __restrict__ pos, int n, int N){
  const bool last = (n == N - 1);
  const Pk8* p = (const Pk8*)(pos + (size_t)n*3 - (last ? 1 : 0));
  unsigned long long v = p->v;
  return last ? (v >> 16) : v;
}

union U4 { uint4 u; bf16x8 b; u16 h[8]; };
union S4 { u32 u[2]; s16x4 v; u16 h[4]; };

__device__ __forceinline__ void lds_barrier(){ asm volatile("s_waitcnt lgkmcnt(0)\n\ts_barrier" ::: "memory"); }
__device__ __forceinline__ void all_barrier(){ asm volatile("s_waitcnt vmcnt(0) lgkmcnt(0)\n\ts_barrier" ::: "memory"); }

struct K1Part { u32 hist[NPMAX]; uint4 stA[CH]; u32 wtot[16]; };

__global__ __launch_bounds__(K1T) void k1_kernel(const u16* __restrict__ h,
    const u16* __restrict__ ea, const int* __restrict__ src, const int* __restrict__ dst,
    const u16* __restrict__ we, const u16* __restrict__ wh,
    u16* __restrict__ Qb, uint4* __restrict__ entA,
    u32* __restrict__ offT, uint4* __restrict__ wF, int N, int E, int nchunk, int ngemm, int np
)
{
  __shared__ __attribute__((aligned(16))) char smem[sizeof(K1Part)];
  const int tid = threadIdx.x;
  const int b = blockIdx.x;
  if (b < nchunk) {
    K1Part& sp = *(K1Part*)smem;
    for (int i = tid; i < np; i += K1T) sp.hist[i] = 0;
    const int base = b * CH;
    int dv[4], sv[4]; uint2 av[4];
#pragma unroll
    for (int k = 0; k < 4; k++) {
      int e = min(base + k*K1T + tid, E-1);
      dv[k] = dst[e];
    }
#pragma unroll
    for (int k = 0; k < 4; k++) {
      int e = min(base + k*K1T + tid, E-1);
      sv[k] = src[e]; av[k] = *(const uint2*)(ea + (size_t)e*4);
    }
    lds_barrier();
    u32 slot[4]; int bn[4];
#pragma unroll
    for (int k = 0; k < 4; k++) {
      int e = base + k*K1T + tid;
      bn[k] = dv[k] / NB; slot[k] = 0;
      if (e < E) slot[k] = atomicAdd(&sp.hist[bn[k]], 1u);
    }
    lds_barrier();
    const int per = (np + K1T - 1) / K1T;
    const int i0 = min(tid * per, np), i1 = min(i0 + per, np);
    u32 loc = 0;
    for (int i = i0; i < i1; i++) loc += sp.hist[i];
    const int l = tid & 63, w = tid >> 6;
    const u32 incl = (u32)wave_scan_incl((int)loc);
    if (l == 63) sp.wtot[w] = incl;
    lds_barrier();
    u32 run = incl - loc;
    {
      const uint4 ta = *(const uint4*)sp.wtot, tb = *(const uint4*)(sp.wtot + 4);
      run += (w > 0 ? ta.x : 0u) + (w > 1 ? ta.y : 0u) + (w > 2 ? ta.z : 0u) + (w > 3 ? ta.w : 0u)
           + (w > 4 ? tb.x : 0u) + (w > 5 ? tb.y : 0u) + (w > 6 ? tb.z : 0u);
    }
    for (int i = i0; i < i1; i++) { u32 c = sp.hist[i]; sp.hist[i] = run; offT[(size_t)i * nchunk + b] = run | (c << 16); run += c; }
    lds_barrier();
#pragma unroll
    for (int k = 0; k < 4; k++) {
      int e = base + k*K1T + tid;
      if (e < E) {
        int idx = sp.hist[bn[k]] + slot[k];
        sp.stA[idx] = make_uint4(av[k].x, av[k].y, (u32)sv[k] | ((u32)(dv[k] - bn[k]*NB) << 26), (u32)e);
      }
    }
    __syncthreads();
    const int ne = min(CH, E - base);
#pragma unroll
    for (int k = 0; k < 4; k++) { int i = tid + k*K1T; if (i < ne) entA[(size_t)base + i] = sp.stA[i]; }
  } else if (b < nchunk + ngemm) {
    u32* Wt32 = (u32*)smem;
    const int w = tid >> 6, l = tid & 63, g = l >> 4, j = l & 15;
    const int rowbase = (b - nchunk) * 64 + (w & 3) * 16;
    const int t0 = (w >> 2) * 4;
    U4 A[4];
    {
      int row = min(rowbase + j, N-1);
#pragma unroll
      for (int s = 0; s < 4; s++) A[s].u = *(const uint4*)(h + (size_t)row*128 + 32*s + 8*g);
    }
    {
      U4 r0[2], r1[2];
#pragma unroll
      for (int i4 = 0; i4 < 2; i4++) {
        int it = tid + i4*K1T;
        int oct = it & 15, kp = it >> 4;
        int k = 128 + 2*kp;
        r0[i4].u = *(const uint4*)(we + k*128 + oct*8);
        r1[i4].u = *(const uint4*)(we + (k+1)*128 + oct*8);
      }
#pragma unroll
      for (int i4 = 0; i4 < 2; i4++) {
        int it = tid + i4*K1T;
        int oct = it & 15, kp = it >> 4;
#pragma unroll
        for (int i = 0; i < 8; i++)
          Wt32[(i*16 + oct)*68 + kp] = (u32)r0[i4].h[i] | ((u32)r1[i4].h[i] << 16);
      }
    }
    __syncthreads();
    f32x4 acc[4];
#pragma unroll
    for (int t = 0; t < 4; t++) acc[t] = (f32x4){0.f,0.f,0.f,0.f};
#pragma unroll
    for (int s = 0; s < 4; s++) {
#pragma unroll
      for (int t = 0; t < 4; t++) {
        U4 B; B.u = *(const uint4*)(Wt32 + ((t0 + t)*16 + j)*68 + 16*s + 4*g);
        acc[t] = __builtin_amdgcn_mfma_f32_16x16x32_bf16(A[s].b, B.b, acc[t], 0, 0, 0);
      }
    }
#pragma unroll
    for (int r = 0; r < 4; r++) {
      int row = rowbase + 4*g + r;
      if (row < N) {
        uint2 v;
        v.x = pk2(acc[0][r], acc[1][r]); v.y = pk2(acc[2][r], acc[3][r]);
        *(uint2*)(Qb + (size_t)row*128 + 8*j + t0) = v;
      }
    }
  } else {
    int it = (b - nchunk - ngemm) * K1T + tid;
    int l = it & 63, f = (it >> 6) & 31, part = it >> 11, t = f & 7, s = f >> 3, g = l >> 4, j = l & 15;
    const u16* wsrc = (part == 0) ? (we + 16*t + j) : (wh + (part - 1)*128*128 + 8*j + t);
    U4 v;
#pragma unroll
    for (int jj = 0; jj < 8; jj++) v.h[jj] = wsrc[(32*s + 8*g + jj)*128];
    wF[it] = v.u;
  }
}

typedef __attribute__((address_space(3))) const void* lds_cptr;
__device__ __forceinline__ u32 lds_addr(const void* p){ return __builtin_amdgcn_readfirstlane((u32)(uintptr_t)(lds_cptr)p); }
__device__ __forceinline__ void dma16(const void* g, u32 lds){
  asm volatile("s_mov_b32 m0, %1\n\ts_nop 0\n\tglobal_load_lds_dwordx4 %0, off" :: "v"(g), "s"(lds) : "memory");
}
__device__ __forceinline__ void dma4(const void* g, u32 lds){
  asm volatile("s_mov_b32 m0, %1\n\ts_nop 0\n\tglobal_load_lds_dword %0, off" :: "v"(g), "s"(lds) : "memory");
}

struct K2Smem {
  uint4 sA[CAP];
  uint4 sW[4096];
  u32   sP[48*128];
  float part[(NB+16)*132];
  uint4 sH[48*16];
  u16   sG[4][CAP];
  u32   sSrc[CAP+4];
  int   ncount[64];
  int   nstart[72];
  int   nrow[64];
  int   flag[4];
  u32   dummy[64];
  u16   posd[128];
  unsigned char sNode[CAP/4];
};

template<bool hostslow>
__global__ __launch_bounds__(K2T) void k2_kernel(const u16* __restrict__ h, const u16* __restrict__ pos,
    const u16* __restrict__ ea, const int* __restrict__ src, const int* __restrict__ dst,
    const u16* __restrict__ we, const u16* __restrict__ wx,
    const u16* __restrict__ Qb,
    const uint4* __restrict__ entA, const u32* __restrict__ offT,
    const uint4* __restrict__ wF,
    u16* __restrict__ out, int N, int E, int nchunk, int np
)
{
  __shared__ __attribute__((aligned(16))) K2Smem sm;
  const int tid = threadIdx.x, w = tid >> 6, l = tid & 63, g = l >> 4, j = l & 15;
  const int blk = blockIdx.x;
  const int n0 = blk * NB;
  const int nn = min(NB, N - n0);
  u16* const sM = (u16*)sm.sA;
  u32* const tmpE = (u32*)sm.part;
  uint4* const uA = (uint4*)(sm.part + 1024);
  u32* const uS = (u32*)(sm.part + 1024 + 4096);
  const int tt = w & 7;
  const int rt0 = (w >> 3) ? 2 : 0, nrt = (w >> 3) ? 1 : 2;

  u32 bposv, d0v, d1v;
  {
    const u16* pb = pos + (size_t)n0*3 + min(tid, nn*3 - 1);
    asm volatile("global_load_ushort %0, %1, off" : "=v"(bposv) : "v"(pb) : "memory");
    const u32* po0 = offT + (hostslow ? (size_t)0 : (size_t)blk * nchunk + min(l, nchunk - 1));
    const u32* po1 = offT + (hostslow ? (size_t)0 : (size_t)blk * nchunk + min(l + 64, nchunk - 1));
    asm volatile("global_load_dword %0, %1, off" : "=v"(d0v) : "v"(po0) : "memory");
    asm volatile("global_load_dword %0, %1, off" : "=v"(d1v) : "v"(po1) : "memory");
  }
#pragma unroll
  for (int k = 0; k < 3; k++) {
    const int id = min(w + 16*k, 43);
    if (id < 12) {
      const int r = id*4 + g;
      const int row = min(n0 + r, N - 1);
      dma16(h + (size_t)row*128 + (j ^ (r & 15))*8, lds_addr(sm.sH + id*64));
    } else {
      dma16(wF + (id - 12)*64 + l, lds_addr(sm.sW + (id - 12)*64));
    }
  }
  for (int i = tid; i < CAP; i += K2T) {
    sm.sA[i] = make_uint4(0,0,0,0); sm.sSrc[i] = 0;
    sm.sG[0][i] = 0; sm.sG[1][i] = 0; sm.sG[2][i] = 0; sm.sG[3][i] = 0;
  }
  if (tid < 64) { sm.ncount[tid] = 0; sm.nrow[tid] = 0; }
  if (tid < 4) { sm.flag[tid] = 0; sm.sSrc[CAP + tid] = 0; }
  asm volatile("s_waitcnt vmcnt(3)" : "+v"(bposv), "+v"(d0v), "+v"(d1v) :: "memory");
  const u16 bpos = (u16)bposv;
  if (hostslow || l >= nchunk) d0v = 0u;
  if (hostslow || l + 64 >= nchunk) d1v = 0u;
  const int cn0 = (int)(d0v >> 16), cn1 = (int)(d1v >> 16);
  const int in0 = wave_scan_incl(cn0);
  const int in1 = wave_scan_incl(cn1) + __builtin_amdgcn_readlane(in0, 63);
  const int T = __builtin_amdgcn_readlane(in1, 63);
  const int sx0 = in0 - cn0, sx1 = in1 - cn1;
  const bool ovf = hostslow || (__ballot(cn0 > 32 || cn1 > 32) != 0ull) || (T > CAP);
  uint4* const cmp = sm.sW + 2048;
  uint4 IA[3]; bool vl[3]; int cp[3];
  const int wu = __builtin_amdgcn_readfirstlane(w);
#pragma unroll
  for (int k = 0; k < 3; k++) {
    const int ca = 2*wu + 32*k, hi = l >> 5, kk = l & 31;
    const u32 da = (u32)__builtin_amdgcn_readlane((int)(k < 2 ? d0v : d1v), ca & 63);
    const u32 db = (u32)__builtin_amdgcn_readlane((int)(k < 2 ? d0v : d1v), (ca + 1) & 63);
    const int sa = __builtin_amdgcn_readlane(k < 2 ? sx0 : sx1, ca & 63);
    const int sb = __builtin_amdgcn_readlane(k < 2 ? sx0 : sx1, (ca + 1) & 63);
    const u32 d = hi ? db : da;
    vl[k] = kk < (int)(d >> 16);
    cp[k] = (hi ? sb : sa) + kk;
    size_t idx = vl[k] ? ((size_t)(ca + hi) * CH + (d & 0xFFFFu) + kk) : (size_t)0;
    IA[k] = entA[idx];
  }
#pragma unroll
  for (int k = 0; k < 3; k++) if (vl[k] && cp[k] < CAP) cmp[cp[k]] = IA[k];
  if (tid < 128) sm.posd[tid] = bpos;
  all_barrier();
  const bool v1 = !ovf && tid < T;
  const uint4 EN = cmp[v1 ? tid : 0];
  const unsigned long long ps_ = hostslow ? 0ull : pos3(pos, v1 ? (int)(EN.z & 0x3FFFFFFu) : 0, N);
  const int dl = v1 ? (int)(EN.z >> 26) : 0;
  int slot = 0;
  if (v1) slot = atomicAdd(&sm.ncount[dl], 1);
  S4 W0[2], W1[2]; float wxv[2];
#pragma unroll
  for (int hh = 0; hh < 2; hh++) {
    int c = 2*l + hh;
#pragma unroll
    for (int k = 0; k < 4; k++) W0[hh].h[k] = we[(256+k)*128 + c];
    W1[hh].u[0] = 0; W1[hh].u[1] = we[260*128 + c];
    wxv[hh] = bf2f(wx[c]);
  }
#pragma unroll
  for (int i = 0; i < 2; i++) {
    if (i < nrt) {
      f32x4 ap = (f32x4){0.f,0.f,0.f,0.f};
      const int r = (rt0 + i)*16 + j;
#pragma unroll
      for (int s = 0; s < 4; s++) {
        U4 A, Be;
        A.u = sm.sH[r*16 + ((4*s + g) ^ j)];
        Be.u = sm.sW[(s*8 + tt)*64 + l];
        ap = __builtin_amdgcn_mfma_f32_16x16x32_bf16(A.b, Be.b, ap, 0, 0, 0);
      }
#pragma unroll
      for (int rr = 0; rr < 4; rr++) {
        float v = ap[rr]; u32 hi = pk2(0.f, v); float lo = v - __uint_as_float(hi & 0xFFFF0000u);
        sm.sP[((rt0+i)*16 + 4*g + rr)*128 + 16*tt + j] = (hi >> 16) | (pk2(0.f, lo) & 0xFFFF0000u);
      }
    }
  }
  lds_barrier();
  int nst;
  {
    int c = (l < NB) ? sm.ncount[l] : 0;
    int cp = (c + 3) & ~3;
    const int incl = wave_scan_incl(cp);
    nst = incl - cp;
    if (w == 0) { sm.nstart[l] = nst; if (l == 63) sm.nstart[64] = incl; }
  }
  const int Tp = __builtin_amdgcn_readlane(nst, 63);
  const bool fast = !ovf && (Tp <= CAP);
  const int nsteps = fast ? (Tp >> 2) : 0;
  if (fast) {
    {
      const int p = __shfl(nst, dl) + slot;
      if (v1) {
        const u16* pd = sm.posd + dl*3;
        float dx = rbf(bf2f(pd[0]) - bf2f((u16)ps_));
        float dy = rbf(bf2f(pd[1]) - bf2f((u16)(ps_ >> 16)));
        float dz = rbf(bf2f(pd[2]) - bf2f((u16)(ps_ >> 32)));
        float d2 = rbf(dx*dx) + rbf(dy*dy) + rbf(dz*dz);
        u32 dxy = pk2(dx, dy), dzd = pk2(dz, d2);
        tmpE[p] = EN.w;
        *(uint4*)&uA[p] = make_uint4((dzd >> 16) | (EN.x << 16), (EN.x >> 16) | (EN.y << 16),
                                     (EN.y >> 16) | (dxy << 16), (dxy >> 16) | (dzd << 16));
        uS[p] = (EN.z & 0x3FFFFFFu) << 8;
      }
    }
    if (w >= 13 && l < NB) {
      const int a = nst, cnt = sm.ncount[l], bnd = a + ((cnt + 3) & ~3);
      if (w == 13) { for (int i = a >> 2; i < (bnd >> 2); i++) sm.sNode[i] = (unsigned char)l; }
      else if (w == 14) { for (int i = a + cnt; i < bnd; i++) tmpE[i] = 0xFFFFFFFFu; }
      else if (cnt > 0) { const int wl = (16*(a >> 2) + 15) / nsteps, wh_ = (16*((bnd >> 2) - 1) + 15) / nsteps; sm.nrow[l] = (l + wl) | ((wh_ - wl + 1) << 16); }
    }
    lds_barrier();
    if (tid < Tp) {
      const u32 ev = tmpE[tid];
      if (ev != 0xFFFFFFFFu) {
        const int n = sm.sNode[tid >> 2];
        const int a = sm.nstart[n], bnd = sm.nstart[n+1];
        const uint4 pa = uA[tid]; const u32 ps = uS[tid];
        int rank = 0;
        for (int i = a; i < bnd; i += 4) {
          uint4 t = *(const uint4*)(tmpE + i);
          rank += (t.x < ev) + (t.y < ev) + (t.z < ev) + (t.w < ev);
        }
        const int p = a + rank;
        sm.sA[p] = make_uint4(pa.x, pa.y, 0x3F803F80u, pa.z & 0xFFFFu);
        sm.sSrc[p] = ps;
        sm.sG[0][p] = 0x3F80; sm.sG[1][p] = (u16)(pa.z >> 16); sm.sG[2][p] = (u16)(pa.w & 0xFFFFu); sm.sG[3][p] = (u16)(pa.w >> 16);
      }
    }
  }
  all_barrier();
  const int st0 = __builtin_amdgcn_readfirstlane((w * nsteps) >> 4);
  const int st1 = __builtin_amdgcn_readfirstlane(((w + 1) * nsteps) >> 4);
  U4 Bh[4], Bm[4];
#pragma unroll
  for (int s = 0; s < 4; s++) { Bh[s].u = wF[2048 + (s*8 + tt)*64 + l]; Bm[s].u = wF[4096 + (s*8 + tt)*64 + l]; }
  if (fast) {
    int cur = -1;
    S4 I4;
    I4.u[0] = ((l & 3) == 0) ? 0x00003F80u : (((l & 3) == 1) ? 0x3F800000u : 0u);
    I4.u[1] = ((l & 3) == 2) ? 0x00003F80u : (((l & 3) == 3) ? 0x3F800000u : 0u);
    f32x4 acc2[2];
    acc2[0] = (f32x4){0.f,0.f,0.f,0.f}; acc2[1] = (f32x4){0.f,0.f,0.f,0.f};
    auto flush = [&](int n) {
      float* pt = sm.part + (n + w) * 132;
      pt[2*l] = acc2[0][0]; pt[2*l + 1] = acc2[1][0];
      float px = acc2[0][1]*wxv[0] + acc2[1][1]*wxv[1];
      float py = acc2[0][2]*wxv[0] + acc2[1][2]*wxv[1];
      float pz = acc2[0][3]*wxv[0] + acc2[1][3]*wxv[1];
      asm volatile(
        "s_nop 1\n\t"
        "v_add_f32_dpp %0, %0, %0 row_shr:1 row_mask:0xf bank_mask:0xf bound_ctrl:1\n\t"
        "v_add_f32_dpp %1, %1, %1 row_shr:1 row_mask:0xf bank_mask:0xf bound_ctrl:1\n\t"
        "v_add_f32_dpp %2, %2, %2 row_shr:1 row_mask:0xf bank_mask:0xf bound_ctrl:1\n\t"
        "v_add_f32_dpp %0, %0, %0 row_shr:2 row_mask:0xf bank_mask:0xf bound_ctrl:1\n\t"
        "v_add_f32_dpp %1, %1, %1 row_shr:2 row_mask:0xf bank_mask:0xf bound_ctrl:1\n\t"
        "v_add_f32_dpp %2, %2, %2 row_shr:2 row_mask:0xf bank_mask:0xf bound_ctrl:1\n\t"
        "v_add_f32_dpp %0, %0, %0 row_shr:4 row_mask:0xf bank_mask:0xf bound_ctrl:1\n\t"
        "v_add_f32_dpp %1, %1, %1 row_shr:4 row_mask:0xf bank_mask:0xf bound_ctrl:1\n\t"
        "v_add_f32_dpp %2, %2, %2 row_shr:4 row_mask:0xf bank_mask:0xf bound_ctrl:1\n\t"
        "v_add_f32_dpp %0, %0, %0 row_shr:8 row_mask:0xf bank_mask:0xf bound_ctrl:1\n\t"
        "v_add_f32_dpp %1, %1, %1 row_shr:8 row_mask:0xf bank_mask:0xf bound_ctrl:1\n\t"
        "v_add_f32_dpp %2, %2, %2 row_shr:8 row_mask:0xf bank_mask:0xf bound_ctrl:1\n\t"
        "v_add_f32_dpp %0, %0, %0 row_bcast:15 row_mask:0xa bank_mask:0xf\n\t"
        "v_add_f32_dpp %1, %1, %1 row_bcast:15 row_mask:0xa bank_mask:0xf\n\t"
        "v_add_f32_dpp %2, %2, %2 row_bcast:15 row_mask:0xa bank_mask:0xf\n\t"
        "v_add_f32_dpp %0, %0, %0 row_bcast:31 row_mask:0xc bank_mask:0xf\n\t"
        "v_add_f32_dpp %1, %1, %1 row_bcast:31 row_mask:0xc bank_mask:0xf\n\t"
        "v_add_f32_dpp %2, %2, %2 row_bcast:31 row_mask:0xc bank_mask:0xf\n\t"
        : "+v"(px), "+v"(py), "+v"(pz));
      if (l == 63) { pt[128] = px; pt[129] = py; pt[130] = pz; }
    };
    const int ndv = (int)sm.sNode[min(st0 + l, CAP/4 - 1)];
    const int ndp = (int)sm.sNode[min(max(st0 + l - 1, 0), CAP/4 - 1)];
    const unsigned long long chg = __ballot(l == 0 || ndv != ndp);
    const u32 ringb = lds_addr(sm.sW + w*256);
    const u32* ringp = (const u32*)(sm.sW + w*256);
    auto qdma = [&](int st, int slot) {
      const u32 so = sm.sSrc[st*4 + (l >> 4)];
      dma16((const char*)Qb + (so + (u32)(l & 15)*16u), ringb + (u32)slot*1024u);
    };
    struct Ops { u32 q0, q1, q2, q3; uint4 a4; uint2 g2; };
    auto rdops = [&](Ops& o, int st) {
      const u32* rp = ringp + ((st - st0) & 3)*256 + l;
      o.q0 = rp[0]; o.q1 = rp[64]; o.q2 = rp[128]; o.q3 = rp[192];
      o.a4 = sm.sA[st*4 + (l & 3)];
      o.g2 = *(const uint2*)(&sm.sG[l & 3][st*4]);
    };
    const int pq1 = st0 + ((st1 - st0) >> 1), pq2 = st0 + (((st1 - st0)*13) >> 4), pq3 = st1 - 1;
    auto body = [&](int st, Ops& c, Ops& nx) {
      if (st == pq1) __builtin_amdgcn_s_setprio(2);
      if (st == pq2) __builtin_amdgcn_s_setprio(1);
      if (st == pq3) __builtin_amdgcn_s_setprio(0);
      if (st + 1 < st1) {
        if (st + 4 < st1) asm volatile("s_waitcnt vmcnt(3)" ::: "memory");
        else              asm volatile("s_waitcnt vmcnt(0)" ::: "memory");
        rdops(nx, st + 1);
      }
      const bool nw = (chg >> (st - st0)) & 1ull;
      if (nw) {
        if (cur >= 0) flush(cur);
        const int n = __builtin_amdgcn_readlane(ndv, st - st0);
        cur = n;
        W1[0].u[0] = sm.sP[n*128 + 2*l]; W1[1].u[0] = sm.sP[n*128 + 2*l + 1];
      }
      S4 B2[2];
      S4 Aa, Ab, A2;
      Aa.u[0] = c.a4.x; Aa.u[1] = c.a4.y; Ab.u[0] = c.a4.z; Ab.u[1] = c.a4.w;
      A2.u[0] = c.g2.x; A2.u[1] = c.g2.y;
#pragma unroll
      for (int hh = 0; hh < 2; hh++) {
        S4 Bq;
        Bq.u[0] = __builtin_amdgcn_perm(c.q1, c.q0, hh ? 0x07060302u : 0x05040100u);
        Bq.u[1] = __builtin_amdgcn_perm(c.q3, c.q2, hh ? 0x07060302u : 0x05040100u);
        f32x4 z = __builtin_amdgcn_mfma_f32_4x4x4bf16_1k(Aa.v, W0[hh].v, (f32x4){0.f,0.f,0.f,0.f}, 0, 0, 0);
        z = __builtin_amdgcn_mfma_f32_4x4x4bf16_1k(Ab.v, W1[hh].v, z, 0, 0, 0);
        z = __builtin_amdgcn_mfma_f32_4x4x4bf16_1k(I4.v, Bq.v, z, 0, 0, 0);
        f32x2 za = {z[0], z[1]}, zb = {z[2], z[3]};
        f32x2 ta = za * -1.4426950408889634f, tb = zb * -1.4426950408889634f;
        f32x2 xa = {__builtin_amdgcn_exp2f(ta[0]), __builtin_amdgcn_exp2f(ta[1])};
        f32x2 xb = {__builtin_amdgcn_exp2f(tb[0]), __builtin_amdgcn_exp2f(tb[1])};
        xa = xa + 1.0f; xb = xb + 1.0f;
        f32x2 ra = {__builtin_amdgcn_rcpf(xa[0]), __builtin_amdgcn_rcpf(xa[1])};
        f32x2 rb = {__builtin_amdgcn_rcpf(xb[0]), __builtin_amdgcn_rcpf(xb[1])};
        za = za * ra; zb = zb * rb;
        B2[hh].u[0] = pk2(za[0], za[1]); B2[hh].u[1] = pk2(zb[0], zb[1]);
      }
      if (nw) {
        acc2[0] = __builtin_amdgcn_mfma_f32_4x4x4bf16_1k(A2.v, B2[0].v, (f32x4){0.f,0.f,0.f,0.f}, 0, 0, 0);
        acc2[1] = __builtin_amdgcn_mfma_f32_4x4x4bf16_1k(A2.v, B2[1].v, (f32x4){0.f,0.f,0.f,0.f}, 0, 0, 0);
      } else {
        acc2[0] = __builtin_amdgcn_mfma_f32_4x4x4bf16_1k(A2.v, B2[0].v, acc2[0], 0, 0, 0);
        acc2[1] = __builtin_amdgcn_mfma_f32_4x4x4bf16_1k(A2.v, B2[1].v, acc2[1], 0, 0, 0);
      }
      if (st + 5 < st1) {
        asm volatile("s_waitcnt lgkmcnt(0)" ::: "memory");
        qdma(st + 5, (st + 1 - st0) & 3);
      }
    };
    __builtin_amdgcn_s_setprio(3);
    if (st0 < st1) {
#pragma unroll
      for (int k = 0; k < 4; k++) if (st0 + k < st1) qdma(st0 + k, k);
      if (st0 + 3 < st1) asm volatile("s_waitcnt vmcnt(3)" ::: "memory");
      else               asm volatile("s_waitcnt vmcnt(0)" ::: "memory");
      Ops X, Y;
      rdops(X, st0);
      if (st0 + 4 < st1) {
        asm volatile("s_waitcnt lgkmcnt(0)" ::: "memory");
        qdma(st0 + 4, 0);
      }
      int st = st0;
      for (; st + 1 < st1; st += 2) { body(st, X, Y); body(st + 1, Y, X); }
      if (st < st1) body(st, X, Y);
    }
    if (cur >= 0) flush(cur);
    __builtin_amdgcn_s_setprio(0);
    lds_barrier();
    for (int i = tid; i < 48*32; i += K2T) {
      int n = i >> 5, c4 = (i & 31) * 4;
      float4 s = make_float4(0.f,0.f,0.f,0.f);
      const int nr = sm.nrow[n];
      for (int rr = 0; rr < (nr >> 16); rr++) {
        float4 v = *(const float4*)(sm.part + ((nr & 0xFFFF) + rr)*132 + c4);
        s.x += v.x; s.y += v.y; s.z += v.z; s.w += v.w;
      }
      uint2 o; o.x = pk2(s.x, s.y); o.y = pk2(s.z, s.w);
      *(uint2*)(sM + n*136 + c4) = o;
    }
    if (tid < nn*3) {
      int n = tid / 3, k = tid - n*3;
      const int nr = sm.nrow[n];
      float s = 0.f;
      for (int rr = 0; rr < (nr >> 16); rr++) s += sm.part[((nr & 0xFFFF) + rr)*132 + 128 + k];
      out[(size_t)N*128 + (size_t)(n0+n)*3 + k] = f2bf(bf2f(bpos) + s);
    }
  } else {
    for (int n = w; n < 48; n += 16) {
      float a0 = 0.f, a1 = 0.f, px = 0.f, py = 0.f, pz = 0.f;
      if (n < nn) {
        const int node = n0 + n;
        const u32 pu0 = sm.sP[n*128 + 2*l], pu1 = sm.sP[n*128 + 2*l + 1];
        const float p0v = bf2f((u16)(pu0 & 0xFFFF)) + bf2f((u16)(pu0 >> 16)), p1v = bf2f((u16)(pu1 & 0xFFFF)) + bf2f((u16)(pu1 >> 16));
        float w50[5], w51[5];
        for (int k = 0; k < 5; k++) { w50[k] = bf2f(we[(256+k)*128 + 2*l]); w51[k] = bf2f(we[(256+k)*128 + 2*l+1]); }
        for (int e0 = 0; e0 < E; e0 += 64) {
          int e = e0 + l;
          int d = (e < E) ? dst[e] : -1;
          unsigned long long m = __ballot(d == node);
          while (m) {
            int bit = __ffsll((long long)m) - 1; m &= m - 1;
            int ee = e0 + bit; int s = src[ee];
            float dx = rbf(bf2f(pos[node*3+0]) - bf2f(pos[s*3+0]));
            float dy = rbf(bf2f(pos[node*3+1]) - bf2f(pos[s*3+1]));
            float dz = rbf(bf2f(pos[node*3+2]) - bf2f(pos[s*3+2]));
            float d2 = rbf(rbf(dx*dx) + rbf(dy*dy) + rbf(dz*dz));
            u32 qq = *(const u32*)(Qb + (size_t)s*128 + 2*l);
            float z0 = p0v + d2*w50[0], z1 = p1v + d2*w51[0];
            for (int k = 0; k < 4; k++) { float f = bf2f(ea[(size_t)ee*4+k]); z0 += f*w50[1+k]; z1 += f*w51[1+k]; }
            z0 += bf2f((u16)(qq & 0xFFFF)); z1 += bf2f((u16)(qq >> 16));
            float m0 = rbf(silu_f(z0)), m1 = rbf(silu_f(z1));
            a0 += m0; a1 += m1;
            float cs = m0*wxv[0] + m1*wxv[1];
            px += dx*cs; py += dy*cs; pz += dz*cs;
          }
        }
        for (int o = 1; o < 64; o <<= 1) { px += __shfl_xor(px, o); py += __shfl_xor(py, o); pz += __shfl_xor(pz, o); }
        if (l == 0) {
          out[(size_t)N*128 + (size_t)node*3 + 0] = f2bf(bf2f(pos[node*3+0]) + px);
          out[(size_t)N*128 + (size_t)node*3 + 1] = f2bf(bf2f(pos[node*3+1]) + py);
          out[(size_t)N*128 + (size_t)node*3 + 2] = f2bf(bf2f(pos[node*3+2]) + pz);
        }
      }
      *(u32*)(sM + n*136 + 2*l) = pk2(a0, a1);
    }
  }
  lds_barrier();
  float* sOut = sm.part;
  {
    f32x4 acc1[2];
    acc1[0] = (f32x4){0.f,0.f,0.f,0.f}; acc1[1] = (f32x4){0.f,0.f,0.f,0.f};
#pragma unroll
    for (int s = 0; s < 4; s++) {
#pragma unroll
      for (int i = 0; i < 2; i++) {
        if (i < nrt) {
          int rl = (rt0 + i)*16 + j;
          U4 Ah, Am;
          Ah.u = sm.sH[rl*16 + ((4*s + g) ^ j)];
          Am.u = *(const uint4*)(sM + rl*136 + 32*s + 8*g);
          acc1[i] = __builtin_amdgcn_mfma_f32_16x16x32_bf16(Ah.b, Bh[s].b, acc1[i], 0, 0, 0);
          acc1[i] = __builtin_amdgcn_mfma_f32_16x16x32_bf16(Am.b, Bm[s].b, acc1[i], 0, 0, 0);
        }
      }
    }
#pragma unroll
    for (int i = 0; i < 2; i++) if (i < nrt) {
#pragma unroll
      for (int r = 0; r < 4; r++) sOut[((rt0+i)*16 + 4*g + r)*132 + 8*j + tt] = acc1[i][r];
    }
  }
  lds_barrier();
  for (int i = tid; i < nn*16; i += K2T) {
    int n = i >> 4, c8 = (i & 15) * 8;
    U4 hv2; hv2.u = sm.sH[n*16 + ((i & 15) ^ (n & 15))];
    U4 o;
#pragma unroll
    for (int k = 0; k < 8; k++) { float z = sOut[n*132 + c8 + k]; o.h[k] = f2bf(bf2f(hv2.h[k]) + silu_f(z)); }
    *(uint4*)(out + (size_t)(n0+n)*128 + c8) = o.u;
  }
}

extern "C" void kernel_launch(void* const* d_in, const int* in_sizes, int n_in,
                              void* d_out, int out_size, void* d_ws, size_t ws_size,
                              hipStream_t stream) {
  const u16* h=(const u16*)d_in[0]; const u16* pos=(const u16*)d_in[1]; const u16* ea=(const u16*)d_in[2];
  const int* src=(const int*)d_in[3]; const int* dst=(const int*)d_in[4];
  const u16* we=(const u16*)d_in[5]; const u16* wx=(const u16*)d_in[6]; const u16* wh=(const u16*)d_in[7];
  int N = in_sizes[0]/128; int E = in_sizes[3];
  int nchunk = (E + CH - 1)/CH; int ngemm = (N + 63)/64; int np = (N + NB - 1)/NB;
  const int hostslow = (np > NPMAX || nchunk > 96 || N > (1 << 24) || N < 2 || E < 1) ? 1 : 0;
  if (hostslow) nchunk = 0;
  char* ws = (char*)d_ws; size_t o = 0;
  auto take = [&](size_t bytes){ void* p = ws + o; o += (bytes + 255) & ~(size_t)255; return p; };
  u16* Qb = (u16*)take((size_t)N*128*2);
  uint4* entA = (uint4*)take((size_t)nchunk*CH*16 + 256);
  u32* offT = (u32*)take((size_t)np*nchunk*4 + 256);
  uint4* wF = (uint4*)take(98304);
  k1_kernel<<<nchunk + ngemm + 12, K1T, 0, stream>>>(h,ea,src,dst,we,wh,Qb,entA,offT,wF,N,E,nchunk,ngemm,np
);
  if (hostslow) k2_kernel<true><<<np, K2T, 0, stream>>>(h,pos,ea,src,dst,we,wx,Qb,entA,offT,wF,(u16*)d_out,N,E,nchunk,np);
  else          k2_kernel<false><<<np, K2T, 0, stream>>>(h,pos,ea,src,dst,we,wx,Qb,entA,offT,wF,(u16*)d_out,N,E,nchunk,np);
}
```
